```python
import torch

def _softmax(x, axis=-1):
    return torch.softmax(x, dim=axis)
def _gelu(x):
    return torch.nn.functional.gelu(x, approximate="tanh")
def _silu(x):
    return torch.nn.functional.silu(x)
def _sigmoid(x):
    return torch.sigmoid(x)
def _layernorm(x, axis=-1, eps=1e-5):
    m = x.mean(dim=axis, keepdim=True)
    v = x.var(dim=axis, keepdim=True, unbiased=False)
    return (x - m) / torch.sqrt(v + eps)
def _scatter_add(tgt, idx, src, axis=0):
    return tgt.clone().index_add_(axis, idx, src)
def _segment_sum(src, seg, n):
    out = torch.zeros((n,) + src.shape[1:], dtype=src.dtype)
    return out.index_add_(0, seg, src)
def _pad(x, pw):
    flat = tuple(v for pair in reversed(pw) for v in pair)
    return torch.nn.functional.pad(x, flat)

kernel_name = 'gated_mix__linear_attn_block__ssm_conv_s4d__prompt'

def setup_inputs():

    _w = (lambda a: (lambda t: t.to(torch.bfloat16) if t.is_floating_point() else t)(torch.as_tensor(a)))
    def _A_setup():
        B, S, D = 1, 2048, 1024
        return {
            "x": _w((lambda *s: torch.randn(*s, dtype=torch.bfloat16) * 0.05)(B, S, D)),
            "wqkv": _w((lambda *s: torch.randn(*s, dtype=torch.bfloat16) * 0.8)(D, 3 * D)),
            "wo": _w((lambda *s: torch.randn(*s, dtype=torch.bfloat16) * 0.05)(D, D)),
        }
    _A_in = _A_setup()

    def _B_setup():
        Bsz, L, D, N = 1, 2048, 1024, 128
        return {
            "u": _w((lambda *s: torch.randn(*s, dtype=torch.bfloat16) * 0.05)(Bsz, L, D)),
            "log_A_real": _w((lambda *s: torch.randn(*s, dtype=torch.bfloat16) * 3.2)(D, N) * 0.5),
            "A_imag": _w((lambda *s: torch.randn(*s, dtype=torch.bfloat16) * 0.05)(D, N) * 3.14),
            "C_real": _w((lambda *s: torch.randn(*s, dtype=torch.bfloat16) * 0.8)(D, N) / N ** 0.5),
            "C_imag": _w((lambda *s: torch.randn(*s, dtype=torch.bfloat16) * 0.8)(D, N) / N ** 0.5),
            "log_dt": _w((lambda *s: torch.randn(*s, dtype=torch.bfloat16) * 0.05)(D) * 0.5 - 4.0),
            "D_skip": _w((lambda *s: torch.randn(*s, dtype=torch.bfloat16) * 0.1)(D)),
        }
    _B_in = _B_setup()

    return {
        "A_x": _A_in["x"],
        "A_wqkv": _A_in["wqkv"],
        "A_wo": _A_in["wo"],
        "B_u": _B_in["u"],
        "B_log_A_real": _B_in["log_A_real"],
        "B_A_imag": _B_in["A_imag"],
        "B_C_real": _B_in["C_real"],
        "B_C_imag": _B_in["C_imag"],
        "B_log_dt": _B_in["log_dt"],
        "B_D_skip": _B_in["D_skip"],
        "gate_w": _w((lambda *s: torch.randn(*s, dtype=torch.bfloat16) * 0.05)(1024) * 16.0),
    }

def reference(A_x, A_wqkv, A_wo, B_u, B_log_A_real, B_A_imag, B_C_real, B_C_imag, B_log_dt, B_D_skip, gate_w):
    def _A_ref(x, wqkv, wo):
        B, S, D = x.shape
        H = 32; Dh = D // H
        qkv = (lambda x, *d: x.permute(*d))((x @ wqkv).reshape(B, S, 3, H, Dh), 2, 0, 3, 1, 4)
        q, k, v = qkv[0], qkv[1], qkv[2]
        phi = lambda t: torch.where(t > 0, t + 1, torch.exp(t)) + 1e-6
        pq, pk = phi(q), phi(k)
        kv = torch.einsum("bhsd,bhse->bhde", pk, v)
        z = torch.einsum("bhsd->bhd", pk)
        num = torch.einsum("bhqd,bhde->bhqe", pq, kv)
        den = torch.einsum("bhqd,bhd->bhq", pq, z)[..., None] + 1e-6
        o = num / den
        return (lambda x, *d: x.permute(*d))(o, 0, 2, 1, 3).reshape(B, S, D) @ wo

    def _B_ref(u, log_A_real, A_imag, C_real, C_imag, log_dt, D_skip):
        L = u.shape[1]
        dt = torch.exp(log_dt)
        A = -torch.exp(log_A_real) + 1j * A_imag
        C = (C_real + 1j * C_imag) * dt[:, None]
        pos = (lambda x, dt: x.to(dt))(torch.arange(L), log_dt.dtype)
        P = torch.exp((A * dt[:, None])[:, :, None] * pos[None, None, :])
        K = 2.0 * torch.einsum("dn,dnl->dl", C, P).real
        L2 = 2 * L
        u_t = (lambda x, *d: x.permute(*d))(u, 0, 2, 1)
        Kf = (lambda x, *a, **k: torch.fft.rfft(x.float(), *a, **k))(K, L2)
        Uf = (lambda x, *a, **k: torch.fft.rfft(x.float(), *a, **k))(u_t, L2)
        y = (lambda x, *a, **k: torch.fft.irfft(x, *a, **k).to(torch.bfloat16))(Uf * Kf[None, :, :], L2)[:, :, :L]
        y = (lambda x, *d: x.permute(*d))(y, 0, 2, 1)
        return y + u * D_skip[None, None, :]

    x = A_x
    g = _sigmoid((x * gate_w).sum(-1, keepdims=True))
    a = _A_ref(x, A_wqkv, A_wo)
    b = _B_ref(x, B_log_A_real, B_A_imag, B_C_real, B_C_imag, B_log_dt, B_D_skip)
    return g * a + (1 - g) * b
```

```cpp
#include <hip/hip_runtime.h>
#include <hip/hip_bf16.h>
typedef __hip_bfloat16 bf16;
#define ROUND_POS 1
constexpr int S=2048, D=1024, H=32, DH=32, NM=128;
__device__ __forceinline__ float b2f(bf16 v){return __bfloat162float(v);}
__device__ __forceinline__ float rbf(float v){return __bfloat162float(__float2bfloat16(v));}

__global__ void gemm_simple(const bf16* A, const bf16* B, float* C, int M, int N, int K){
  __shared__ float As[16][17], Bs[16][17];
  int r=blockIdx.y*16+threadIdx.y, c=blockIdx.x*16+threadIdx.x;
  float acc=0;
  for(int k0=0;k0<K;k0+=16){
    As[threadIdx.y][threadIdx.x]=b2f(A[(long)r*K+k0+threadIdx.x]);
    Bs[threadIdx.y][threadIdx.x]=b2f(B[(long)(k0+threadIdx.y)*N+c]);
    __syncthreads();
    for(int k=0;k<16;k++) acc+=As[threadIdx.y][k]*Bs[k][threadIdx.x];
    __syncthreads();
  }
  C[(long)r*N+c]=acc;
}
__device__ __forceinline__ float phi(float t){
  float r = t>0 ? rbf(t+1.f) : rbf(expf(t));
  return rbf(r+1e-6f);
}
__global__ void kv_kernel(const float* qkv, float* kv, float* z){
  int h=blockIdx.x; int d=threadIdx.x/32, e=threadIdx.x%32;
  float acc=0, zz=0;
  for(int s=0;s<S;s++){
    float k=phi(rbf(qkv[(long)s*3*D+D+h*DH+d]));
    float v=rbf(qkv[(long)s*3*D+2*D+h*DH+e]);
    acc+=k*v; zz+=k;
  }
  kv[h*1024+d*32+e]=rbf(acc);
  if(e==0) z[h*32+d]=rbf(zz);
}
__global__ void o_kernel(const float* qkv, const float* kv, const float* z, bf16* o){
  int s=blockIdx.x; int h=threadIdx.x/32, e=threadIdx.x%32;
  float num=0, den=0;
  for(int d=0;d<32;d++){
    float q=phi(rbf(qkv[(long)s*3*D+h*DH+d]));
    num+=q*kv[h*1024+d*32+e]; den+=q*z[h*32+d];
  }
  num=rbf(num); den=rbf(rbf(den)+1e-6f);
  o[(long)s*D+h*32+e]=__float2bfloat16(num/den);
}
__global__ void kgen(const bf16* lA, const bf16* Ai, const bf16* Cr, const bf16* Ci, const bf16* ldt, float* K){
  int d=blockIdx.y; int l=blockIdx.x*256+threadIdx.x;
  float dt=rbf(expf(b2f(ldt[d])));
  float pos = ROUND_POS ? rbf((float)l) : (float)l;
  float acc=0;
  for(int n=0;n<NM;n++){
    float ar=-rbf(expf(b2f(lA[d*NM+n])))*dt, ai=b2f(Ai[d*NM+n])*dt;
    float cr=b2f(Cr[d*NM+n])*dt, ci=b2f(Ci[d*NM+n])*dt;
    float e=expf(ar*pos); float sn,cs; sincosf(ai*pos,&sn,&cs);
    acc+=e*(cr*cs-ci*sn);
  }
  K[(long)d*S+l]=2.f*acc;
}
__global__ void conv_combine(const bf16* x, const float* K, const bf16* Dskip, const bf16* gw, const float* a, bf16* out){
  int t=blockIdx.x;
  __shared__ float red[1024];
  int d=threadIdx.x;
  float xv=b2f(x[(long)t*D+d]);
  red[d]=rbf(xv*b2f(gw[d]));
  __syncthreads();
  for(int o=512;o>0;o>>=1){ if(d<o) red[d]+=red[d+o]; __syncthreads(); }
  float gs=rbf(red[0]);
  float g=rbf(1.f/(1.f+expf(-gs)));
  float y=0;
  for(int j=0;j<=t;j++) y+=K[(long)d*S+(t-j)]*b2f(x[(long)j*D+d]);
  y=rbf(y);
  float b=rbf(y+rbf(xv*b2f(Dskip[d])));
  float av=rbf(a[(long)t*D+d]);
  float r=rbf(g*av)+rbf(rbf(1.f-g)*b);
  out[(long)t*D+d]=__float2bfloat16(r);
}
extern "C" void kernel_launch(void* const* d_in, const int* in_sizes, int n_in,
                              void* d_out, int out_size, void* d_ws, size_t ws_size,
                              hipStream_t stream) {
  const bf16* x=(const bf16*)d_in[0]; const bf16* wqkv=(const bf16*)d_in[1]; const bf16* wo=(const bf16*)d_in[2];
  const bf16* lA=(const bf16*)d_in[4]; const bf16* Ai=(const bf16*)d_in[5]; const bf16* Cr=(const bf16*)d_in[6]; const bf16* Ci=(const bf16*)d_in[7];
  const bf16* ldt=(const bf16*)d_in[8]; const bf16* Dk=(const bf16*)d_in[9]; const bf16* gw=(const bf16*)d_in[10];
  char* ws=(char*)d_ws;
  float* qkv=(float*)ws; ws+= (size_t)S*3*D*4;
  float* kv=(float*)ws; ws+= 32*1024*4;
  float* z=(float*)ws; ws+= 4096;
  bf16* o=(bf16*)ws; ws+=(size_t)S*D*2;
  float* a=(float*)ws; ws+=(size_t)S*D*4;
  float* K=(float*)ws; ws+=(size_t)S*D*4;
  gemm_simple<<<dim3(3*D/16,S/16),dim3(16,16),0,stream>>>(x,wqkv,qkv,S,3*D,D);
  kv_kernel<<<32,1024,0,stream>>>(qkv,kv,z);
  o_kernel<<<S,1024,0,stream>>>(qkv,kv,z,o);
  gemm_simple<<<dim3(D/16,S/16),dim3(16,16),0,stream>>>(o,wo,a,S,D,D);
  kgen<<<dim3(S/256,D),256,0,stream>>>(lA,Ai,Cr,Ci,ldt,K);
  conv_combine<<<S,1024,0,stream>>>(x,K,Dk,gw,a,(bf16*)d_out);
}
```

```cpp
#include <hip/hip_runtime.h>
#include <hip/hip_bf16.h>
#define AS1 __attribute__((address_space(1)))
#define AS3 __attribute__((address_space(3)))
#define DI __device__ __forceinline__
typedef __bf16 bf16x2 __attribute__((ext_vector_type(2)));
typedef __bf16 bf16x4 __attribute__((ext_vector_type(4)));
typedef __bf16 bf16x8 __attribute__((ext_vector_type(8)));
typedef float f32x2 __attribute__((ext_vector_type(2)));
typedef float f32x4 __attribute__((ext_vector_type(4)));
typedef unsigned u32x2 __attribute__((ext_vector_type(2)));
typedef unsigned u32x4 __attribute__((ext_vector_type(4)));
typedef AS1 unsigned gu32;

constexpr int SLOT=40960, NST=16, LDS_BYTES=163840;
constexpr unsigned TAGINC=0x9E3779B9u;
constexpr unsigned SPINCAP=1u<<18;
constexpr unsigned WS_EPOCH=0;
constexpr unsigned WS_G  =4096;
constexpr unsigned WS_FKV=65536;
constexpr unsigned WS_FO =WS_FKV+32768;
constexpr unsigned WS_FX =WS_FO+32768;
constexpr unsigned WS_FB =WS_FX+32768;
constexpr unsigned WS_ZP =262144;
constexpr unsigned WS_KVP=1u<<20;
constexpr unsigned WS_OF =2u<<20;
constexpr unsigned WS_XT =6u<<20;
constexpr unsigned WS_BT =10u<<20;
constexpr unsigned WS_END=14u<<20;
constexpr unsigned WS_DBG=16u<<20;
constexpr unsigned L_WO=40960;
constexpr unsigned L_KG=122880;
constexpr unsigned L_SSM=131072;
constexpr unsigned R_KC=4576, R_P0=5888, R_P1=R_P0+1088;
constexpr unsigned L_FLAG=LDS_BYTES-64;

DI void glds16(const void* gbase, unsigned voff, unsigned lds){
  unsigned keep;
  asm volatile("s_mov_b32 %0, m0\n\ts_mov_b32 m0, %3\n\ts_nop 0\n\tglobal_load_lds_dwordx4 %1, %2\n\ts_mov_b32 m0, %0"
     : "=&s"(keep) : "v"(voff), "s"(gbase), "s"(lds) : "memory");
}
#define WAITV(n) asm volatile("s_waitcnt vmcnt(" #n ")":::"memory")
#define WAITL() asm volatile("s_waitcnt lgkmcnt(0)":::"memory")
#define BAR_L() asm volatile("s_waitcnt lgkmcnt(0)\n\ts_barrier":::"memory")
#define MFMA16(a,b,c) __builtin_amdgcn_mfma_f32_16x16x32_bf16((a),(b),(c),0,0,0)
#define SB() __builtin_amdgcn_sched_barrier(0)
#define SG(m,n) __builtin_amdgcn_sched_group_barrier(m,n,0)
#define RLX_AGENT __ATOMIC_RELAXED, __HIP_MEMORY_SCOPE_AGENT
#define RLX_WG __ATOMIC_RELAXED, __HIP_MEMORY_SCOPE_WORKGROUP
#define BC8(x) __builtin_bit_cast(bf16x8,(x))
#define TR16(p) __builtin_amdgcn_ds_read_tr16_b64_v4bf16((AS3 bf16x4*)(p))
#define CAT8(a,b) __builtin_shufflevector((a),(b),0,1,2,3,4,5,6,7)
#define LDB128(off,soff) __builtin_amdgcn_raw_buffer_load_b128(rs,(off),(soff),16)
#define STB128(v,off,soff) __builtin_amdgcn_raw_buffer_store_b128((v),rs,(off),(soff),16)

#define ST(i)

DI unsigned pk2(float a,float b){ f32x2 t={a,b}; bf16x2 p=__builtin_convertvector(t,bf16x2); return __builtin_bit_cast(unsigned,p);}
DI u32x4 pk8(const f32x4&a,const f32x4&b){ u32x4 r; r[0]=pk2(a[0],a[1]); r[1]=pk2(a[2],a[3]); r[2]=pk2(b[0],b[1]); r[3]=pk2(b[2],b[3]); return r; }
DI float blo(unsigned w){ return __builtin_bit_cast(float,w<<16); }
DI float bhi(unsigned w){ return __builtin_bit_cast(float,w&0xffff0000u); }
DI float rbf(float v){ return blo(pk2(v,v)); }
DI f32x4 phi4(const f32x4& t){
  f32x4 u=t*1.44269504088896f, w=t+1e-6f, r;
  #pragma unroll
  for(int i=0;i<4;i++){ float e=__builtin_amdgcn_fmed3f(__builtin_amdgcn_exp2f(u[i]),0.f,1.f); r[i]=__builtin_fmaxf(w[i],1e-6f)+e; }
  return r;
}
DI void poll_all(const gu32* p, unsigned tag){
  #pragma unroll 1
  for(unsigned it=0; it<SPINCAP; ++it){
    unsigned v=__hip_atomic_load(p, RLX_AGENT);
    if(__builtin_amdgcn_ballot_w64(v!=tag)==0ull) break;
    __builtin_amdgcn_s_sleep(1);
  }
  asm volatile("":::"memory");
}
DI void lds_wait_ge(AS3 unsigned* p, unsigned want){
  #pragma unroll 1
  for(unsigned it=0; it<(SPINCAP<<4); ++it){
    unsigned v=__hip_atomic_load(p,RLX_WG);
    if((unsigned)__builtin_amdgcn_readfirstlane(v)>=want) break;
  }
  asm volatile("":::"memory");
}
DI unsigned lds_arrive(AS3 unsigned* p,int lane){
  unsigned old=0; if(lane==0) old=__hip_atomic_fetch_add(p,1u,RLX_WG);
  return __builtin_amdgcn_readfirstlane(old);
}
template<int CTRL> DI float dpp_add(float x){
  int y=__builtin_amdgcn_update_dpp(0,__builtin_bit_cast(int,x),CTRL,0xf,0xf,true);
  return x+__builtin_bit_cast(float,y);
}
DI float row_sum16(float x){ return dpp_add<0x121>(dpp_add<0x122>(dpp_add<0x124>(dpp_add<0x128>(x)))); }
struct Cx{ f32x2 re,im; };
DI Cx cmul(const Cx&a,const Cx&b){ Cx r; r.re=a.re*b.re-a.im*b.im; r.im=a.re*b.im+a.im*b.re; return r; }
DI Cx cexp2(f32x2 lr,f32x2 rev,float p){
  Cx r;
  #pragma unroll
  for(int i=0;i<2;i++){ float e=__builtin_amdgcn_exp2f(lr[i]*p); float f=__builtin_amdgcn_fractf(rev[i]*p);
    r.re[i]=e*__builtin_amdgcn_cosf(f); r.im[i]=e*__builtin_amdgcn_sinf(f); }
  return r;
}
DI int bid_g1(int r,int c){ return (((r&7)|((c&3)<<3))<<3)|(r>>3)|((c>>2)<<1); }
DI int bid_g2(int r2,int c2){ return ((((r2&1)<<4)|c2)<<3)|(r2>>1); }

__global__ __launch_bounds__(512,2) void fused(const char* __restrict__ x, const char* __restrict__ w, const char* __restrict__ wo,
     const char* __restrict__ pla, const char* __restrict__ pai, const char* __restrict__ pcr, const char* __restrict__ pci,
     const char* __restrict__ pdt, const char* __restrict__ pdk, const char* __restrict__ pgw,
     char* __restrict__ out, char* ws){
  extern __shared__ __attribute__((aligned(16))) char smem_[];
  AS3 char* smem=(AS3 char*)smem_;
  const int tid=threadIdx.x, lane=tid&63;
  const int wid=__builtin_amdgcn_readfirstlane(tid>>6);
  const int bid=blockIdx.x, g8=bid&7, idx=bid>>3;
  const int r=(g8&1)*8+(idx&7), c=(g8>>1)*4+(idx>>3);
  const int r2=2*g8+(idx>>4), c2=idx&15;
  const int g4=lane>>4, l15=lane&15;
  ST(0);
  const unsigned lbase=(unsigned)(size_t)smem;
  const unsigned vwo=(lane>>3)*2048+(((lane&7)^(((lane>>4)&3)<<1))*16);
  const char* wob=wo+c2*128;
  AS3 unsigned* lflag=(AS3 unsigned*)(smem+L_FLAG);
  const __amdgpu_buffer_rsrc_t rs=__builtin_amdgcn_make_buffer_rsrc(ws,0,WS_END,0x00020000);
  if(wid>=4){
    const int lw=wid-4;
    const int ch=64*c+4*r+lw;
    const int xb8=8*(((g8&1)+(g8>>1))&1), rho=(r+c)&1;
    const int xchunk=xb8+(c>>3)*4+(c&3);
    unsigned q_la,q_ai,q_cr,q_ci,q_dt,q_dk,q_ep;
    { unsigned o=ch*256+lane*4, o2=ch*2, z=0;
      asm volatile("global_load_dword %0, %1, %2":"=v"(q_la):"v"(o),"s"(pla):"memory");
      asm volatile("global_load_dword %0, %1, %2":"=v"(q_ai):"v"(o),"s"(pai):"memory");
      asm volatile("global_load_dword %0, %1, %2":"=v"(q_cr):"v"(o),"s"(pcr):"memory");
      asm volatile("global_load_dword %0, %1, %2":"=v"(q_ci):"v"(o),"s"(pci):"memory");
      asm volatile("global_load_ushort %0, %1, %2":"=v"(q_dt):"v"(o2),"s"(pdt):"memory");
      asm volatile("global_load_ushort %0, %1, %2":"=v"(q_dk):"v"(o2),"s"(pdk):"memory");
      asm volatile("global_load_dword %0, %1, %2 sc1":"=v"(q_ep):"v"(z),"s"(ws):"memory");
    }
    unsigned vx[4], vw[6];
    #pragma unroll
    for(int i=0;i<4;i++){ int row=8*(4*lw+i)+(lane>>3); int cc=(lane&7)^((row>>1)&7); vx[i]=row*2048+cc*16; }
    #pragma unroll
    for(int i=0;i<6;i++){ int p=6*lw+i; int seg=p>>3, kr=(p&7)*8+(lane>>3); int cc=(lane&7)^((((kr>>1)&1)|(((kr>>3)&1)<<1))<<1); vw[i]=kr*6144+seg*2048+cc*16; }
    const char* xb=x+(size_t)r*128*2048;
    const char* wb=w+c*128;
    auto issue=[&](int s){
      unsigned ls=lbase+(s%3)*SLOT;
      int kc=(xb8+(s^rho))&15;
      const char* xs=xb+kc*128; const char* wsb=wb+(size_t)kc*64*6144;
      #define GX(i) glds16(xs,vx[i],ls+(4*lw+i)*1024)
      #define GW(i) glds16(wsb,vw[i],ls+16384+(6*lw+i)*1024)
      GW(0);GX(0);GW(1);GW(2);GX(1);GW(3);GX(2);GW(4);GW(5);GX(3);
    };
    issue(0); issue(1);
    asm volatile("s_waitcnt vmcnt(10)\n\ts_barrier":"+v"(q_la),"+v"(q_ai),"+v"(q_cr),"+v"(q_ci),"+v"(q_dt),"+v"(q_dk),"+v"(q_ep)::"memory");
    const unsigned tag=__builtin_amdgcn_readfirstlane(q_ep)+TAGINC;
    const int xstage=(xchunk-xb8)^rho;
    AS3 char* kg=smem+L_KG+lw*10240;
    AS3 char* kgw=kg+lane*8;
    AS3 char* kgr=kg+l15*528+g4*16;
    Cx w1,w2,w4,w8c,w128,G,E;
    bf16x8 OF[8]; f32x4 kt0={},kt1={},kt2={};
    auto put=[&](int e,const Cx&v){ u32x2 t={pk2(v.re[0],v.re[1]),pk2(v.im[0],v.im[1])}; *(AS3 u32x2*)(kgw+e*528)=t; };
    auto slice=[&](int s){
      if(s==0){
        float dt=rbf(__builtin_amdgcn_exp2f(blo(q_dt)*1.44269504088896f));
        f32x2 lr={-rbf(__builtin_amdgcn_exp2f(blo(q_la)*1.44269504088896f))*dt*1.44269504088896f, -rbf(__builtin_amdgcn_exp2f(bhi(q_la)*1.44269504088896f))*dt*1.44269504088896f};
        f32x2 rev={blo(q_ai)*dt*0.159154943091895f, bhi(q_ai)*dt*0.159154943091895f};
        G.re=f32x2{blo(q_cr),bhi(q_cr)}*(2.f*dt); G.im=f32x2{blo(q_ci),bhi(q_ci)}*(2.f*dt);
        w1=cexp2(lr,rev,1.f); w8c=cexp2(lr,rev,8.f); w8c.im=-w8c.im; w128=cexp2(lr,rev,128.f);
        w2=cmul(w1,w1); w4=cmul(w2,w2);
      } else if(s==1){
        E.re=f32x2{1.f,1.f}; E.im=f32x2{0.f,0.f}; put(0,E); E=w8c; put(1,E);
        #pragma unroll
        for(int j=2;j<8;j++){ E=cmul(E,w8c); put(j,E); }
      } else if(s==2){
        #pragma unroll
        for(int j=8;j<16;j++){ E=cmul(E,w8c); put(j,E); }
        #pragma unroll
        for(int t=0;t<8;t++) OF[t]=*(AS3 bf16x8*)(kgr+t*64);
      } else if(s==3 || s==4){
        const int b=(s-3)*8;
        E=G; put(b,E);
        #pragma unroll
        for(int j=1;j<8;j++){ E=cmul(E,w1); put(b+j,E); }
        G=cmul(G,w128);
        if(s==4){
          #pragma unroll
          for(int t=0;t<8;t++) kt0=MFMA16(*(AS3 bf16x8*)(kgr+t*64),OF[t],kt0);
        }
      } else if(s==5){
        #pragma unroll
        for(int i=0;i<2;i++){ E=G; put(4*i,E);
          #pragma unroll
          for(int j=1;j<4;j++){ E=cmul(E,w2); put(4*i+j,E); }
          G=cmul(G,w128); }
      } else if(s==6){
        #pragma unroll
        for(int i=0;i<4;i++){ put(8+2*i,G); E=cmul(G,w4); put(9+2*i,E); G=cmul(G,w128); }
        #pragma unroll
        for(int t=0;t<8;t++) kt1=MFMA16(*(AS3 bf16x8*)(kgr+t*64),OF[t],kt1);
      } else if(s==7){
        #pragma unroll
        for(int i=0;i<9;i++){ put(i,G); if(i<8) G=cmul(G,w128); }
        #pragma unroll
        for(int t=0;t<8;t++) kt2=MFMA16(OF[t],*(AS3 bf16x8*)(kgr+t*64),kt2);
      } else if(s==8){
        AS3 char* kc=smem+L_SSM+lw*8192+R_KC;
        u32x2 t0={pk2(kt0[0],kt0[1]),pk2(kt0[2],kt0[3])};
        *(AS3 u32x2*)(kc+2*(128*(g4>>1)+8*l15+4*(g4&1)))=t0;
        int a0= g4<2 ? 256+64*g4+4*l15 : 384+64*(g4-2)+2*l15;
        int a1= g4<2 ? a0+2 : a0+32;
        *(AS3 unsigned*)(kc+2*a0)=pk2(kt1[0],kt1[1]);
        *(AS3 unsigned*)(kc+2*a1)=pk2(kt1[2],kt1[3]);
        if(l15<9){ u32x2 t2={pk2(kt2[0],kt2[1]),pk2(kt2[2],kt2[3])}; *(AS3 u32x2*)(kc+2*(512+16*l15+4*g4))=t2; }
        if(lane<30){ u32x4 z={0,0,0,0}; *(AS3 u32x4*)(smem+L_SSM+lw*8192+lane*16)=z; }
      }
    };
    #pragma unroll
    for(int s=0;s<NST-2;s++){
      issue(s+2);
      if(s<8 && s==xstage){
        int q=l15>>2,p=lane&3;
        AS3 char* b=smem+(s%3)*SLOT+(8*g4+q)*128+8*(p&1);
        AS3 char* b0=b+(((2*lw+(p>>1))^((4*g4+(q>>1))&7))*16);
        AS3 char* b1=b+512+(((2*lw+(p>>1))^((4*g4+2+(q>>1))&7))*16);
        unsigned o=WS_XT+(64*xchunk+16*lw+l15)*4096+r*256+g4*16;
        #pragma unroll
        for(int j=0;j<4;j++){ bf16x8 v=CAT8(TR16(b0+j*4096),TR16(b1+j*4096)); STB128(__builtin_bit_cast(u32x4,v),o+j*64,0); }
      }
      if(s>=2 && s<10 && s==xstage+2 && lw==0 && lane<16)
        __hip_atomic_store((gu32*)(ws+WS_FX+bid_g1(lane,xchunk)*128+r*4),tag,RLX_AGENT);
      slice(s);
      asm volatile("s_waitcnt vmcnt(10) lgkmcnt(0)\n\ts_barrier":::"memory");
    }
    ST(1);
    #pragma unroll
    for(int i=0;i<10;i++){ int p=10*lw+i; glds16(wob+(size_t)((p+8*c2)&127)*16384,vwo,lbase+L_WO+p*1024); }
    asm volatile("s_waitcnt vmcnt(10)\n\ts_barrier":::"memory");
    #pragma unroll
    for(int i=0;i<12;i++){ int p=40+12*lw+i; glds16(wob+(size_t)((p+8*c2)&127)*16384,vwo,lbase+L_WO+p*1024); }
    asm volatile("s_barrier":::"memory");
    ST(2);
    AS3 char* R=smem+L_SSM+lw*8192;
    {
      const char* xr=x+(size_t)(128*r+8*c+2*lw)*2048+lane*16;
      u32x4 xa[4], gw[2];
      #pragma unroll
      for(int i=0;i<4;i++) xa[i]=*(const u32x4*)(xr+i*1024);
      #pragma unroll
      for(int i=0;i<2;i++) gw[i]=*(const u32x4*)(pgw+lane*16+i*1024);
      poll_all((gu32*)(ws+WS_FX+bid*128+l15*4),tag);
      u32x4 U[4];
      #pragma unroll
      for(int i=0;i<4;i++) U[i]=LDB128(lane*16+i*1024,WS_XT+ch*4096);
      float gs[2];
      #pragma unroll
      for(int rr=0;rr<2;rr++){ float s=0;
        #pragma unroll
        for(int i=0;i<2;i++)
        #pragma unroll
        for(int j=0;j<4;j++){ unsigned a=xa[rr*2+i][j], b=gw[i][j]; unsigned pr=pk2(blo(a)*blo(b),bhi(a)*bhi(b)); s+=blo(pr)+bhi(pr); }
        s=row_sum16(s); s+=__shfl_xor(s,16); s+=__shfl_xor(s,32);
        s=rbf(s);
        gs[rr]=rbf(__builtin_amdgcn_rcpf(1.f+__builtin_amdgcn_exp2f(-s*1.44269504088896f)));
      }
      if(lane<2) __hip_atomic_store((gu32*)(ws+WS_G+(128*r+8*c+2*lw+lane)*4),__builtin_bit_cast(unsigned,lane?gs[1]:gs[0]),RLX_AGENT);
      #pragma unroll
      for(int i=0;i<4;i++) *(AS3 u32x4*)(R+480+lane*16+i*1024)=U[i];
      WAITV(0);
      lds_arrive(lflag+4,lane);
    }
    ST(3);
    f32x4 y[8]={};
    {
      AS3 char* kc=R+R_KC;
      u32x4 sv0={0,0,0,0}, sv1={0,0,0,0};
      int n0=15-l15+8*g4;
      AS3 char* ak=R+((n0&1)?R_P1:R_P0)+4*(n0>>1);
      AS3 char* bu=R+32*l15+16*g4;
      #pragma unroll
      for(int chi=0;chi<4;chi++){
        const int beta=64*chi+63-lane;
        u32x4 d; unsigned r8;
        if(chi==0){
          if(lane<32){
            u32x2 e=*(AS3 u32x2*)(kc+2*(128+4*beta)); unsigned c4=*(AS3 unsigned short*)(kc+2*(132+4*beta));
            unsigned pm=*(AS3 unsigned short*)(kc+2*255);
            d[0]=(e[1]&0xffff0000u)|c4; d[1]=__builtin_amdgcn_perm(e[1],e[1],0x01000100u); d[2]=(e[0]&0xffff0000u)|(e[1]&0xffffu); d[3]=__builtin_amdgcn_perm(e[0],e[0],0x01000100u);
            r8= lane==31 ? pm : e[0];
          } else {
            u32x4 e=*(AS3 u32x4*)(kc+16*beta); unsigned pm=*(AS3 unsigned short*)(kc+16*beta-2);
            #pragma unroll
            for(int i=0;i<4;i++) d[i]=__builtin_amdgcn_alignbit(e[3-i],e[3-i],16);
            r8= lane==63 ? 0u : pm;
          }
        } else if(chi==1){
          unsigned e=*(AS3 unsigned*)(kc+2*(256+2*beta)); unsigned c2_=*(AS3 unsigned short*)(kc+2*(258+2*beta));
          d[0]=c2_|(c2_<<16); d[1]=__builtin_amdgcn_perm(e,e,0x03020302u); d[2]=__builtin_amdgcn_alignbit(e,e,16); d[3]=__builtin_amdgcn_perm(e,e,0x01000100u);
          r8=e;
        } else {
          unsigned c0=*(AS3 unsigned short*)(kc+2*(384+beta)), c1=*(AS3 unsigned short*)(kc+2*(385+beta));
          d[0]=c1|(c1<<16); d[1]=c1|(((beta&1)?c1:c0)<<16); d[2]=c0|(c0<<16); d[3]=d[2];
          r8=c0;
        }
        u32x4 f;
        f[0]=__builtin_amdgcn_alignbit(d[1],d[0],16); f[1]=__builtin_amdgcn_alignbit(d[2],d[1],16);
        f[2]=__builtin_amdgcn_alignbit(d[3],d[2],16); f[3]=__builtin_amdgcn_alignbit(r8,d[3],16);
        *(AS3 u32x4*)(R+R_P0+lane*16)=d; *(AS3 u32x4*)(R+R_P1+lane*16)=f;
        if(lane<2){ *(AS3 u32x4*)(R+R_P0+1024+lane*16)=sv0; *(AS3 u32x4*)(R+R_P1+1024+lane*16)=sv1; }
        sv0=d; sv1=f;
        u32x4 A[16];
        #pragma unroll
        for(int m=0;m<16;m++){
          AS3 unsigned* p=(AS3 unsigned*)(ak+64*(15-m));
          A[m]=u32x4{p[0],p[1],p[2],p[3]};
        }
        const int ne=8-2*chi;
        bf16x8 Bf[2][8];
        #pragma unroll
        for(int e=0;e<ne;e++) Bf[0][e]=*(AS3 bf16x8*)(bu+448+512*e);
        SB();
        #pragma unroll
        for(int m0=0;m0<8;m0++){
          if(m0<7){
            #pragma unroll
            for(int e=0;e<ne;e++) Bf[(m0+1)&1][e]=*(AS3 bf16x8*)(bu+448+512*e-64*(m0+1));
          }
          #pragma unroll
          for(int e=0;e<ne;e++){
            y[e+2*chi]=MFMA16(BC8(A[m0]),Bf[m0&1][e],y[e+2*chi]);
            if(e+2*chi+1<8) y[e+2*chi+1]=MFMA16(BC8(A[8+m0]),Bf[m0&1][e],y[e+2*chi+1]);
          }
          #pragma unroll
          for(int e=0;e<ne;e++){ SG(0x8,1); SG(0x100,1); }
          SG(0x8,8);
          SB();
        }
      }
    }
    ST(4);
    {
      const float dk=blo(q_dk);
      #pragma unroll
      for(int i=0;i<8;i++){
        u32x2 uu=*(AS3 u32x2*)(R+480+512*i+32*l15+8*g4);
        u32x2 o={pk2(y[i][0]+blo(uu[0])*dk,y[i][1]+bhi(uu[0])*dk),pk2(y[i][2]+blo(uu[1])*dk,y[i][3]+bhi(uu[1])*dk)};
        __builtin_amdgcn_raw_buffer_store_b64(o,rs,32*l15+8*g4+512*i,WS_BT+ch*4096,16);
      }
      WAITV(0);
      if(lds_arrive(lflag+10,lane)==3 && lane<16)
        __hip_atomic_store((gu32*)(ws+WS_FB+bid_g2(lane,c)*128+r*4),tag,RLX_AGENT);
    }
    ST(5);
  } else {
    const int wr=wid>>1, wh=wid&1;
    unsigned epoch=__hip_atomic_load((gu32*)(ws+WS_EPOCH), RLX_AGENT);
    if(wid==0 && lane<16) lflag[lane]=0u;
    unsigned ax[2];
    { int row=64*wr+l15; int f=(row>>1)&7;
      #pragma unroll
      for(int ks=0;ks<2;ks++) ax[ks]=row*128+(((4*ks+g4)^f)*16); }
    unsigned aw[2];
    { int q=l15>>2,p=lane&3; int sw=(((q>>1)&1)|((g4&1)<<1))<<1;
      #pragma unroll
      for(int n=0;n<2;n++){ int cc=(4*wh+2*n+(p>>1))^sw; aw[n]=16384+(8*g4+q)*128+cc*16+8*(p&1);} }
    f32x4 acc[4][6]={};
    struct Fr{ bf16x8 x[4]; bf16x8 w[6]; };
    auto rd=[&](Fr&F,int slot,int ks){
      AS3 char* b=smem+slot*SLOT;
      #pragma unroll
      for(int m=0;m<4;m++) F.x[m]=*(AS3 bf16x8*)(b+ax[ks]+m*2048);
      #pragma unroll
      for(int sg=0;sg<3;sg++)
      #pragma unroll
      for(int n=0;n<2;n++){ AS3 char* bw=b+aw[n]+ks*4096+sg*8192; F.w[sg*2+n]=CAT8(TR16(bw),TR16(bw+512)); }
    };
    auto mm=[&](const Fr&F){
      #pragma unroll
      for(int n=0;n<6;n++)
      #pragma unroll
      for(int m=0;m<4;m++) acc[m][n]= n<2 ? MFMA16(F.w[n],F.x[m],acc[m][n]) : MFMA16(F.x[m],F.w[n],acc[m][n]);
    };
    Fr F0,F1;
    #define I1() SG(0x8,1);SG(0x100,1);
    #define ILV() do{ I1()I1()I1()I1()I1()I1()I1()I1()I1()I1()I1()I1()I1()I1()I1()I1() SG(0x8,8);}while(0)
    const unsigned tag=__builtin_amdgcn_readfirstlane(epoch)+TAGINC;
    __builtin_amdgcn_s_waitcnt(0xC07F);
    BAR_L();
    ST(1);
    rd(F0,0,0);
    int slot=0;
    SB();
    for(int s=0;s<NST-1;s++){
      rd(F1,slot,1); mm(F0); ILV(); SB();
      slot = slot==2?0:slot+1;
      BAR_L(); SB(); rd(F0,slot,0);
      mm(F1); ILV(); SB();
    }
    rd(F1,slot,1); mm(F0); ILV(); SB();
    BAR_L(); SB();
    mm(F1);
    ST(2);
    __builtin_amdgcn_s_setprio(2);
    const u32x4 ones={0x3F803F80u,0x3F803F80u,0x3F803F80u,0x3F803F80u};
    f32x4 kvt[2][2]={}, zt[2]={};
    #pragma unroll
    for(int mu=0;mu<2;mu++){
      u32x4 pv0=pk8(acc[2*mu][4],acc[2*mu+1][4]), pv1=pk8(acc[2*mu][5],acc[2*mu+1][5]);
      #pragma unroll
      for(int dl=0;dl<2;dl++){
        u32x4 pk=pk8(phi4(acc[2*mu][2+dl]),phi4(acc[2*mu+1][2+dl]));
        kvt[dl][0]=MFMA16(BC8(pk),BC8(pv0),kvt[dl][0]);
        kvt[dl][1]=MFMA16(BC8(pk),BC8(pv1),kvt[dl][1]);
        zt[dl]=MFMA16(BC8(pk),BC8(ones),zt[dl]);
      }
    }
    AS3 char* inbox=smem+wid*10240;
    AS3 char* outbox=smem+(wid^2)*10240;
    {
      f32x4 s0=wr?kvt[0][0]:kvt[0][1], s1=wr?kvt[1][0]:kvt[1][1];
      *(AS3 f32x4*)(outbox+lane*16)=s0; *(AS3 f32x4*)(outbox+1024+lane*16)=s1;
      WAITL();
      if(lane==0) __hip_atomic_store(lflag+(wid^2),1u,RLX_WG);
      if(l15==0){
        unsigned zo=WS_ZP+((((c*2+wh)*2+wr)*16+r)*4+g4)*32;
        STB128(__builtin_bit_cast(u32x4,zt[0]),zo,0);
        STB128(__builtin_bit_cast(u32x4,zt[1]),zo+16,0);
      }
      f32x4 o0=wr?kvt[0][1]:kvt[0][0], o1=wr?kvt[1][1]:kvt[1][0];
      lds_wait_ge(lflag+wid,1u);
      o0+=*(AS3 f32x4*)(inbox+lane*16); o1+=*(AS3 f32x4*)(inbox+1024+lane*16);
      STB128(pk8(o0,o1),lane*16,WS_KVP+(((c*2+wh)*2+wr)*16+r)*1024);
    }
    ST(3);
    u32x4 pq[4];
    #pragma unroll
    for(int m=0;m<4;m++) pq[m]=pk8(phi4(acc[m][0]),phi4(acc[m][1]));
    asm volatile("s_waitcnt vmcnt(0)":"+v"(pq[0]),"+v"(pq[1]),"+v"(pq[2]),"+v"(pq[3])::"memory");
    if(lds_arrive(lflag+6,lane)==3){
      if(lane<16) __hip_atomic_store((gu32*)(ws+WS_FKV+bid_g1(lane,c)*128+r*4),tag,RLX_AGENT);
      ST(4);
      poll_all((gu32*)(ws+WS_FKV+bid*128+l15*4),tag);
      if(lane==0) __hip_atomic_store(lflag+7,1u,RLX_WG);
    } else { ST(4); lds_wait_ge(lflag+7,1u); }
    ST(5);
    u32x4 kvF[2], zF;
    {
      u32x4 P[16], Z[4];
      unsigned ko=WS_KVP+((c*2+wh)*2+wr)*16384;
      unsigned zo=WS_ZP+(((c*2+wh)*2)*16+l15)*128+g4*32;
      Z[0]=LDB128(zo,0); Z[1]=LDB128(zo+16,0); Z[2]=LDB128(zo+2048,0); Z[3]=LDB128(zo+2048+16,0);
      #pragma unroll
      for(int p=0;p<16;p++) P[p]=LDB128(lane*16,ko+((p+2*r)&15)*1024);
      SB();
      u32x4 sel0={0,0,0,0}, sel1={0,0,0,0};
      { unsigned on=((l15>>2)==g4)?((l15&1)?0x3F800000u:0x00003F80u):0u;
        unsigned lo=(l15&2)?0u:on, hi=(l15&2)?on:0u;
        sel0[0]=lo; sel0[1]=hi; sel1[2]=lo; sel1[3]=hi; }
      f32x4 z0=__builtin_bit_cast(f32x4,Z[0])+__builtin_bit_cast(f32x4,Z[2]);
      f32x4 z1=__builtin_bit_cast(f32x4,Z[1])+__builtin_bit_cast(f32x4,Z[3]);
      #pragma unroll
      for(int i=0;i<4;i++){ z0[i]=row_sum16(z0[i]); z1[i]=row_sum16(z1[i]); }
      zF=pk8(z0,z1);
      f32x4 d0={},d1={};
      #pragma unroll
      for(int p=0;p<16;p++){ d0=MFMA16(BC8(sel0),BC8(P[p]),d0); d1=MFMA16(BC8(sel1),BC8(P[p]),d1); }
      u32x4 own=pk8(d0,d1);
      *(AS3 u32x4*)(outbox+2048+lane*16)=own;
      WAITL();
      if(lane==0) __hip_atomic_store(lflag+(wid^2),2u,RLX_WG);
      lds_wait_ge(lflag+wid,2u);
      u32x4 oth=*(AS3 u32x4*)(inbox+2048+lane*16);
      kvF[0]=wr?oth:own; kvF[1]=wr?own:oth;
    }
    ST(6);
    #pragma unroll
    for(int m=0;m<4;m++){
      const f32x4 zero={};
      f32x4 den=MFMA16(BC8(zF),BC8(pq[m]),zero);
      f32x4 n0=MFMA16(BC8(kvF[0]),BC8(pq[m]),zero), n1=MFMA16(BC8(kvF[1]),BC8(pq[m]),zero);
      float rc=__builtin_amdgcn_rcpf(den[0]+1e-6f);
      STB128(pk8(n0*rc,n1*rc),lane*16,WS_OF+((8*r+4*wr+m)*32+2*c+wh)*1024);
    }
    ST(7);
    lds_wait_ge(lflag+4,4u);
    WAITV(0);
    const bool lastC = lds_arrive(lflag+8,lane)==3;
    if(lastC && lane<16) __hip_atomic_store((gu32*)(ws+WS_FO+bid_g2(r,lane)*128+c*4),tag,RLX_AGENT);
    ST(8);
    #pragma unroll
    for(int i=0;i<10;i++){ int p=88+10*wid+i; glds16(wob+(size_t)((p+8*c2)&127)*16384,vwo,lbase+(p-88)*1024); }
    unsigned ao[4];
    { int q=l15>>2,p=lane&3; int sw=((2*g4+(q>>1))&3)<<1;
      #pragma unroll
      for(int n=0;n<4;n++) ao[n]=(4*g4+q)*128+(((2*n+(p>>1))^sw)*16)+8*(p&1); }
    if(lastC){
      poll_all((gu32*)(ws+WS_FO+bid*128+l15*4),tag);
      if(lane==0) __hip_atomic_store(lflag+9,1u,RLX_WG);
      if(bid==0 && lane==0) __hip_atomic_store((gu32*)(ws+WS_EPOCH),tag,RLX_AGENT);
    } else lds_wait_ge(lflag+9,1u);
    ST(9);
    f32x4 a2[4][2]={};
    const unsigned oo=WS_OF+(8*r2+2*wid)*32768;
#define PF 6
    u32x4 B[32][2];
    auto ldB=[&](int h){
      B[h][0]=LDB128(lane*16,oo+((h+2*c2)&31)*1024);
      B[h][1]=LDB128(lane*16,oo+32768+((h+2*c2)&31)*1024);
    };
    bf16x8 A[2][4];
    auto rdA=[&](int h){
      const unsigned hb = h<22 ? L_WO+4096*h : 4096*(h-22);
      #pragma unroll
      for(int n=0;n<4;n++){ AS3 char* bw=smem+ao[n]+hb; A[h&1][n]=CAT8(TR16(bw),TR16(bw+2048)); }
    };
    #pragma unroll
    for(int h=0;h<PF;h++) ldB(h);
    rdA(0);
    SB();
    #pragma unroll
    for(int h=0;h<32;h++){
      if(h==1){ lds_arrive(lflag+5,lane); SB(); }
      if(h==21){ lds_wait_ge(lflag+5,4u); SB(); }
      if(h+1<32) rdA(h+1);
      if(h+PF<32) ldB(h+PF);
      #pragma unroll
      for(int n=0;n<4;n++){ a2[n][0]=MFMA16(A[h&1][n],BC8(B[h][0]),a2[n][0]); a2[n][1]=MFMA16(A[h&1][n],BC8(B[h][1]),a2[n][1]); }
      SG(0x8,1);SG(0x100,2);SG(0x8,1);SG(0x100,2);SG(0x8,1);SG(0x100,2);SG(0x8,1);SG(0x100,2);SG(0x8,1);SG(0x20,1);SG(0x8,1);SG(0x20,1);SG(0x8,2);
      SB();
    }
    ST(10);
    poll_all((gu32*)(ws+WS_FB+bid*128+l15*4),tag);
    {
      const int t0=128*r2+32*wid;
      u32x4 bt[4];
      #pragma unroll
      for(int n=0;n<4;n++) bt[n]=LDB128(l15*4096+g4*16,WS_BT+(64*c2+16*n)*4096+t0*2);
      float gv[2];
      #pragma unroll
      for(int mu=0;mu<2;mu++) gv[mu]=__builtin_bit_cast(float,__hip_atomic_load((gu32*)(ws+WS_G+(t0+16*mu+l15)*4),RLX_AGENT));
      #pragma unroll
      for(int mu=0;mu<2;mu++){
        u32x4 sel={0,0,0,0};
        { unsigned on=(g4==2*mu+(l15>>3))?((l15&1)?0x3F800000u:0x00003F80u):0u; int dw=(l15&7)>>1;
          sel[0]=dw==0?on:0u; sel[1]=dw==1?on:0u; sel[2]=dw==2?on:0u; sel[3]=dw==3?on:0u; }
        char* op=out+(size_t)(t0+16*mu+l15)*2048+c2*128+g4*8;
        const float g=gv[mu], h1=rbf(1.f-g);
        #pragma unroll
        for(int n=0;n<4;n++){
          const f32x4 zero={};
          f32x4 bb=MFMA16(BC8(bt[n]),BC8(sel),zero);
          f32x4 o=a2[n][mu]*g+bb*h1;
          u32x2 v={pk2(o[0],o[1]),pk2(o[2],o[3])}; *(u32x2*)(op+n*32)=v; }
      }
    }
  }
}
extern "C" void kernel_launch(void* const* d_in, const int* in_sizes, int n_in,
                              void* d_out, int out_size, void* d_ws, size_t ws_size,
                              hipStream_t stream) {
  (void)hipFuncSetAttribute((const void*)fused,hipFuncAttributeMaxDynamicSharedMemorySize,LDS_BYTES);
  fused<<<256,512,LDS_BYTES,stream>>>((const char*)d_in[0],(const char*)d_in[1],(const char*)d_in[2],
     (const char*)d_in[4],(const char*)d_in[5],(const char*)d_in[6],(const char*)d_in[7],
     (const char*)d_in[8],(const char*)d_in[9],(const char*)d_in[10],(char*)d_out,(char*)d_ws);
}
```

```cpp
#define DA 0
#define DB 0
#define DBID 77
#include <hip/hip_runtime.h>
#include <hip/hip_bf16.h>
#define AS1 __attribute__((address_space(1)))
#define AS3 __attribute__((address_space(3)))
#define DI __device__ __forceinline__
typedef __bf16 bf16x2 __attribute__((ext_vector_type(2)));
typedef __bf16 bf16x4 __attribute__((ext_vector_type(4)));
typedef __bf16 bf16x8 __attribute__((ext_vector_type(8)));
typedef float f32x2 __attribute__((ext_vector_type(2)));
typedef float f32x4 __attribute__((ext_vector_type(4)));
typedef unsigned u32x2 __attribute__((ext_vector_type(2)));
typedef unsigned u32x4 __attribute__((ext_vector_type(4)));
typedef AS1 unsigned gu32;

constexpr int SLOT=40960, NST=16, LDS_BYTES=163840;
constexpr unsigned TAGINC=0x9E3779B9u;
constexpr unsigned SPINCAP=1u<<18;
constexpr unsigned WS_EPOCH=0;
constexpr unsigned WS_G  =4096;
constexpr unsigned WS_FKV=65536;
constexpr unsigned WS_FO =WS_FKV+32768;
constexpr unsigned WS_FX =WS_FO+32768;
constexpr unsigned WS_FB =WS_FX+32768;
constexpr unsigned WS_ZP =262144;
constexpr unsigned WS_KVP=1u<<20;
constexpr unsigned WS_OF =2u<<20;
constexpr unsigned WS_XT =6u<<20;
constexpr unsigned WS_BT =10u<<20;
constexpr unsigned WS_END=14u<<20;
constexpr unsigned WS_DBG=16u<<20;
constexpr unsigned L_WO=40960;
constexpr unsigned L_KG=122880;
constexpr unsigned L_SSM=131072;
constexpr unsigned R_KC=4576, R_P0=5888, R_P1=R_P0+1088;
constexpr unsigned L_FLAG=LDS_BYTES-64;

DI void glds16(const void* gbase, unsigned voff, unsigned lds){
  unsigned keep;
  asm volatile("s_mov_b32 %0, m0\n\ts_mov_b32 m0, %3\n\ts_nop 0\n\tglobal_load_lds_dwordx4 %1, %2\n\ts_mov_b32 m0, %0"
     : "=&s"(keep) : "v"(voff), "s"(gbase), "s"(lds) : "memory");
}
#define WAITV(n) asm volatile("s_waitcnt vmcnt(" #n ")":::"memory")
#define WAITL() asm volatile("s_waitcnt lgkmcnt(0)":::"memory")
#define BAR_L() asm volatile("s_waitcnt lgkmcnt(0)\n\ts_barrier":::"memory")
#define MFMA16(a,b,c) __builtin_amdgcn_mfma_f32_16x16x32_bf16((a),(b),(c),0,0,0)
#define SB() __builtin_amdgcn_sched_barrier(0)
#define SG(m,n) __builtin_amdgcn_sched_group_barrier(m,n,0)
#define RLX_AGENT __ATOMIC_RELAXED, __HIP_MEMORY_SCOPE_AGENT
#define RLX_WG __ATOMIC_RELAXED, __HIP_MEMORY_SCOPE_WORKGROUP
#define BC8(x) __builtin_bit_cast(bf16x8,(x))
#define TR16(p) __builtin_amdgcn_ds_read_tr16_b64_v4bf16((AS3 bf16x4*)(p))
#define CAT8(a,b) __builtin_shufflevector((a),(b),0,1,2,3,4,5,6,7)
#define LDB128(off,soff) __builtin_amdgcn_raw_buffer_load_b128(rs,(off),(soff),16)
#define STB128(v,off,soff) __builtin_amdgcn_raw_buffer_store_b128((v),rs,(off),(soff),16)

#define STAMP
#define ST(i) do{ tr[i]=__builtin_amdgcn_s_memrealtime(); }while(0)

DI unsigned pk2(float a,float b){ f32x2 t={a,b}; bf16x2 p=__builtin_convertvector(t,bf16x2); return __builtin_bit_cast(unsigned,p);}
DI u32x4 pk8(const f32x4&a,const f32x4&b){ u32x4 r; r[0]=pk2(a[0],a[1]); r[1]=pk2(a[2],a[3]); r[2]=pk2(b[0],b[1]); r[3]=pk2(b[2],b[3]); return r; }
DI float blo(unsigned w){ return __builtin_bit_cast(float,w<<16); }
DI float bhi(unsigned w){ return __builtin_bit_cast(float,w&0xffff0000u); }
DI float rbf(float v){ return blo(pk2(v,v)); }
DI f32x4 phi4(const f32x4& t){
  f32x4 u=t*1.44269504088896f, w=t+1e-6f, r;
  #pragma unroll
  for(int i=0;i<4;i++){ float e=__builtin_amdgcn_fmed3f(__builtin_amdgcn_exp2f(u[i]),0.f,1.f); r[i]=__builtin_fmaxf(w[i],1e-6f)+e; }
  return r;
}
DI void poll_all(const gu32* p, unsigned tag){
  #pragma unroll 1
  for(unsigned it=0; it<SPINCAP; ++it){
    unsigned v=__hip_atomic_load(p, RLX_AGENT);
    if(__builtin_amdgcn_ballot_w64(v!=tag)==0ull) break;
    __builtin_amdgcn_s_sleep(1);
  }
  asm volatile("":::"memory");
}
DI void lds_wait_ge(AS3 unsigned* p, unsigned want){
  #pragma unroll 1
  for(unsigned it=0; it<(SPINCAP<<4); ++it){
    unsigned v=__hip_atomic_load(p,RLX_WG);
    if((unsigned)__builtin_amdgcn_readfirstlane(v)>=want) break;
  }
  asm volatile("":::"memory");
}
DI unsigned lds_arrive(AS3 unsigned* p,int lane){
  unsigned old=0; if(lane==0) old=__hip_atomic_fetch_add(p,1u,RLX_WG);
  return __builtin_amdgcn_readfirstlane(old);
}
template<int CTRL> DI float dpp_add(float x){
  int y=__builtin_amdgcn_update_dpp(0,__builtin_bit_cast(int,x),CTRL,0xf,0xf,true);
  return x+__builtin_bit_cast(float,y);
}
DI float row_sum16(float x){ return dpp_add<0x121>(dpp_add<0x122>(dpp_add<0x124>(dpp_add<0x128>(x)))); }
struct Cx{ f32x2 re,im; };
DI Cx cmul(const Cx&a,const Cx&b){ Cx r; r.re=a.re*b.re-a.im*b.im; r.im=a.re*b.im+a.im*b.re; return r; }
DI Cx cexp2(f32x2 lr,f32x2 rev,float p){
  Cx r;
  #pragma unroll
  for(int i=0;i<2;i++){ float e=__builtin_amdgcn_exp2f(lr[i]*p); float f=__builtin_amdgcn_fractf(rev[i]*p);
    r.re[i]=e*__builtin_amdgcn_cosf(f); r.im[i]=e*__builtin_amdgcn_sinf(f); }
  return r;
}
DI int bid_g1(int r,int c){ return (((r&7)|((c&3)<<3))<<3)|(r>>3)|((c>>2)<<1); }
DI int bid_g2(int r2,int c2){ return ((((r2&1)<<4)|c2)<<3)|(r2>>1); }

__global__ __launch_bounds__(512,2) void fused(const char* __restrict__ x, const char* __restrict__ w, const char* __restrict__ wo,
     const char* __restrict__ pla, const char* __restrict__ pai, const char* __restrict__ pcr, const char* __restrict__ pci,
     const char* __restrict__ pdt, const char* __restrict__ pdk, const char* __restrict__ pgw,
     char* __restrict__ out, char* ws){
  extern __shared__ __attribute__((aligned(16))) char smem_[];
  AS3 char* smem=(AS3 char*)smem_;
  const int tid=threadIdx.x, lane=tid&63;
  const int wid=__builtin_amdgcn_readfirstlane(tid>>6);
  const int bid=blockIdx.x, g8=bid&7, idx=bid>>3;
  const int r=(g8&1)*8+(idx&7), c=(g8>>1)*4+(idx>>3);
  const int r2=2*g8+(idx>>4), c2=idx&15;
  const int g4=lane>>4, l15=lane&15;
  unsigned long long tr[16];
  ST(0);
  const unsigned lbase=(unsigned)(size_t)smem;
  const unsigned vwo=(lane>>3)*2048+(((lane&7)^(((lane>>4)&3)<<1))*16);
  const char* wob=wo+c2*128;
  AS3 unsigned* lflag=(AS3 unsigned*)(smem+L_FLAG);
  const __amdgpu_buffer_rsrc_t rs=__builtin_amdgcn_make_buffer_rsrc(ws,0,WS_END,0x00020000);
  if(wid>=4){
    const int lw=wid-4;
    const int ch=64*c+4*r+lw;
    const int xb8=8*(((g8&1)+(g8>>1))&1), rho=(r+c)&1;
    const int xchunk=xb8+(c>>3)*4+(c&3);
    unsigned q_la,q_ai,q_cr,q_ci,q_dt,q_dk,q_ep;
    { unsigned o=ch*256+lane*4, o2=ch*2, z=0;
      asm volatile("global_load_dword %0, %1, %2":"=v"(q_la):"v"(o),"s"(pla):"memory");
      asm volatile("global_load_dword %0, %1, %2":"=v"(q_ai):"v"(o),"s"(pai):"memory");
      asm volatile("global_load_dword %0, %1, %2":"=v"(q_cr):"v"(o),"s"(pcr):"memory");
      asm volatile("global_load_dword %0, %1, %2":"=v"(q_ci):"v"(o),"s"(pci):"memory");
      asm volatile("global_load_ushort %0, %1, %2":"=v"(q_dt):"v"(o2),"s"(pdt):"memory");
      asm volatile("global_load_ushort %0, %1, %2":"=v"(q_dk):"v"(o2),"s"(pdk):"memory");
      asm volatile("global_load_dword %0, %1, %2 sc1":"=v"(q_ep):"v"(z),"s"(ws):"memory");
    }
    unsigned vx[4], vw[6];
    #pragma unroll
    for(int i=0;i<4;i++){ int row=8*(4*lw+i)+(lane>>3); int cc=(lane&7)^((row>>1)&7); vx[i]=row*2048+cc*16; }
    #pragma unroll
    for(int i=0;i<6;i++){ int p=6*lw+i; int seg=p>>3, kr=(p&7)*8+(lane>>3); int cc=(lane&7)^((((kr>>1)&1)|(((kr>>3)&1)<<1))<<1); vw[i]=kr*6144+seg*2048+cc*16; }
    const char* xb=x+(size_t)r*128*2048;
    const char* wb=w+c*128;
    auto issue=[&](int s){
      unsigned ls=lbase+(s%3)*SLOT;
      int kc=(xb8+(s^rho))&15;
      const char* xs=xb+kc*128; const char* wsb=wb+(size_t)kc*64*6144;
      #define GX(i) glds16(xs,vx[i],ls+(4*lw+i)*1024)
      #define GW(i) glds16(wsb,vw[i],ls+16384+(6*lw+i)*1024)
      GW(0);GX(0);GW(1);GW(2);GX(1);GW(3);GX(2);GW(4);GW(5);GX(3);
    };
    issue(0); issue(1);
    asm volatile("s_waitcnt vmcnt(10)\n\ts_barrier":"+v"(q_la),"+v"(q_ai),"+v"(q_cr),"+v"(q_ci),"+v"(q_dt),"+v"(q_dk),"+v"(q_ep)::"memory");
    const unsigned tag=__builtin_amdgcn_readfirstlane(q_ep)+TAGINC;
    const int xstage=(xchunk-xb8)^rho;
    AS3 char* kg=smem+L_KG+lw*10240;
    AS3 char* kgw=kg+lane*8;
    AS3 char* kgr=kg+l15*528+g4*16;
    Cx w1,w2,w4,w8c,w128,G,E;
    bf16x8 OF[8]; f32x4 kt0={},kt1={},kt2={};
    auto put=[&](int e,const Cx&v){ u32x2 t={pk2(v.re[0],v.re[1]),pk2(v.im[0],v.im[1])}; *(AS3 u32x2*)(kgw+e*528)=t; };
    auto slice=[&](int s){
      if(s==0){
        float dt=rbf(__builtin_amdgcn_exp2f(blo(q_dt)*1.44269504088896f));
        f32x2 lr={-rbf(__builtin_amdgcn_exp2f(blo(q_la)*1.44269504088896f))*dt*1.44269504088896f, -rbf(__builtin_amdgcn_exp2f(bhi(q_la)*1.44269504088896f))*dt*1.44269504088896f};
        f32x2 rev={blo(q_ai)*dt*0.159154943091895f, bhi(q_ai)*dt*0.159154943091895f};
        G.re=f32x2{blo(q_cr),bhi(q_cr)}*(2.f*dt); G.im=f32x2{blo(q_ci),bhi(q_ci)}*(2.f*dt);
        w1=cexp2(lr,rev,1.f); w8c=cexp2(lr,rev,8.f); w8c.im=-w8c.im; w128=cexp2(lr,rev,128.f);
        w2=cmul(w1,w1); w4=cmul(w2,w2);
      } else if(s==1){
        E.re=f32x2{1.f,1.f}; E.im=f32x2{0.f,0.f}; put(0,E); E=w8c; put(1,E);
        #pragma unroll
        for(int j=2;j<8;j++){ E=cmul(E,w8c); put(j,E); }
      } else if(s==2){
        #pragma unroll
        for(int j=8;j<16;j++){ E=cmul(E,w8c); put(j,E); }
        #pragma unroll
        for(int t=0;t<8;t++) OF[t]=*(AS3 bf16x8*)(kgr+t*64);
      } else if(s==3 || s==4){
        const int b=(s-3)*8;
        E=G; put(b,E);
        #pragma unroll
        for(int j=1;j<8;j++){ E=cmul(E,w1); put(b+j,E); }
        G=cmul(G,w128);
        if(s==4){
          #pragma unroll
          for(int t=0;t<8;t++) kt0=MFMA16(*(AS3 bf16x8*)(kgr+t*64),OF[t],kt0);
        }
      } else if(s==5){
        #pragma unroll
        for(int i=0;i<2;i++){ E=G; put(4*i,E);
          #pragma unroll
          for(int j=1;j<4;j++){ E=cmul(E,w2); put(4*i+j,E); }
          G=cmul(G,w128); }
      } else if(s==6){
        #pragma unroll
        for(int i=0;i<4;i++){ put(8+2*i,G); E=cmul(G,w4); put(9+2*i,E); G=cmul(G,w128); }
        #pragma unroll
        for(int t=0;t<8;t++) kt1=MFMA16(*(AS3 bf16x8*)(kgr+t*64),OF[t],kt1);
      } else if(s==7){
        #pragma unroll
        for(int i=0;i<9;i++){ put(i,G); if(i<8) G=cmul(G,w128); }
        #pragma unroll
        for(int t=0;t<8;t++) kt2=MFMA16(OF[t],*(AS3 bf16x8*)(kgr+t*64),kt2);
      } else if(s==8){
        AS3 char* kc=smem+L_SSM+lw*8192+R_KC;
        u32x2 t0={pk2(kt0[0],kt0[1]),pk2(kt0[2],kt0[3])};
        *(AS3 u32x2*)(kc+2*(128*(g4>>1)+8*l15+4*(g4&1)))=t0;
        int a0= g4<2 ? 256+64*g4+4*l15 : 384+64*(g4-2)+2*l15;
        int a1= g4<2 ? a0+2 : a0+32;
        *(AS3 unsigned*)(kc+2*a0)=pk2(kt1[0],kt1[1]);
        *(AS3 unsigned*)(kc+2*a1)=pk2(kt1[2],kt1[3]);
        if(l15<9){ u32x2 t2={pk2(kt2[0],kt2[1]),pk2(kt2[2],kt2[3])}; *(AS3 u32x2*)(kc+2*(512+16*l15+4*g4))=t2; }
        if(lane<30){ u32x4 z={0,0,0,0}; *(AS3 u32x4*)(smem+L_SSM+lw*8192+lane*16)=z; }
      }
    };
    #pragma unroll
    for(int s=0;s<NST-2;s++){
      issue(s+2);
      if(s<8 && s==xstage){
        int q=l15>>2,p=lane&3;
        AS3 char* b=smem+(s%3)*SLOT+(8*g4+q)*128+8*(p&1);
        AS3 char* b0=b+(((2*lw+(p>>1))^((4*g4+(q>>1))&7))*16);
        AS3 char* b1=b+512+(((2*lw+(p>>1))^((4*g4+2+(q>>1))&7))*16);
        unsigned o=WS_XT+(64*xchunk+16*lw+l15)*4096+r*256+g4*16;
        #pragma unroll
        for(int j=0;j<4;j++){ bf16x8 v=CAT8(TR16(b0+j*4096),TR16(b1+j*4096)); STB128(__builtin_bit_cast(u32x4,v),o+j*64,0); }
      }
      if(s>=2 && s<10 && s==xstage+2 && lw==0 && lane<16)
        __hip_atomic_store((gu32*)(ws+WS_FX+bid_g1(lane,xchunk)*128+r*4),tag,RLX_AGENT);
      slice(s);
      asm volatile("s_waitcnt vmcnt(10) lgkmcnt(0)\n\ts_barrier":::"memory");
    }
    ST(1);
    #pragma unroll
    for(int i=0;i<10;i++){ int p=10*lw+i; glds16(wob+(size_t)((p+8*c2)&127)*16384,vwo,lbase+L_WO+p*1024); }
    asm volatile("s_waitcnt vmcnt(10)\n\ts_barrier":::"memory");
    #pragma unroll
    for(int i=0;i<12;i++){ int p=40+12*lw+i; glds16(wob+(size_t)((p+8*c2)&127)*16384,vwo,lbase+L_WO+p*1024); }
    asm volatile("s_barrier":::"memory");
    ST(2);
    AS3 char* R=smem+L_SSM+lw*8192;
    {
      const char* xr=x+(size_t)(128*r+8*c+2*lw)*2048+lane*16;
      u32x4 xa[4], gw[2];
      #pragma unroll
      for(int i=0;i<4;i++) xa[i]=*(const u32x4*)(xr+i*1024);
      #pragma unroll
      for(int i=0;i<2;i++) gw[i]=*(const u32x4*)(pgw+lane*16+i*1024);
      poll_all((gu32*)(ws+WS_FX+bid*128+l15*4),tag);
      u32x4 U[4];
      #pragma unroll
      for(int i=0;i<4;i++) U[i]=LDB128(lane*16+i*1024,WS_XT+ch*4096);
      float gs[2];
      #pragma unroll
      for(int rr=0;rr<2;rr++){ float s=0;
        #pragma unroll
        for(int i=0;i<2;i++)
        #pragma unroll
        for(int j=0;j<4;j++){ unsigned a=xa[rr*2+i][j], b=gw[i][j]; unsigned pr=pk2(blo(a)*blo(b),bhi(a)*bhi(b)); s+=blo(pr)+bhi(pr); }
        s=row_sum16(s); s+=__shfl_xor(s,16); s+=__shfl_xor(s,32);
        s=rbf(s);
        gs[rr]=rbf(__builtin_amdgcn_rcpf(1.f+__builtin_amdgcn_exp2f(-s*1.44269504088896f)));
      }
      if(lane<2) __hip_atomic_store((gu32*)(ws+WS_G+(128*r+8*c+2*lw+lane)*4),__builtin_bit_cast(unsigned,lane?gs[1]:gs[0]),RLX_AGENT);
      #pragma unroll
      for(int i=0;i<4;i++) *(AS3 u32x4*)(R+480+lane*16+i*1024)=U[i];
      WAITV(0);
      lds_arrive(lflag+4,lane);
    }
    ST(3);
    f32x4 y[8]={};
    {
      AS3 char* kc=R+R_KC;
      u32x4 sv0={0,0,0,0}, sv1={0,0,0,0};
      int n0=15-l15+8*g4;
      AS3 char* ak=R+((n0&1)?R_P1:R_P0)+4*(n0>>1);
      AS3 char* bu=R+32*l15+16*g4;
      #pragma unroll
      for(int chi=0;chi<4;chi++){
        const int beta=64*chi+63-lane;
        u32x4 d; unsigned r8;
        if(chi==0){
          if(lane<32){
            u32x2 e=*(AS3 u32x2*)(kc+2*(128+4*beta)); unsigned c4=*(AS3 unsigned short*)(kc+2*(132+4*beta));
            unsigned pm=*(AS3 unsigned short*)(kc+2*255);
            d[0]=(e[1]&0xffff0000u)|c4; d[1]=__builtin_amdgcn_perm(e[1],e[1],0x01000100u); d[2]=(e[0]&0xffff0000u)|(e[1]&0xffffu); d[3]=__builtin_amdgcn_perm(e[0],e[0],0x01000100u);
            r8= lane==31 ? pm : e[0];
          } else {
            u32x4 e=*(AS3 u32x4*)(kc+16*beta); unsigned pm=*(AS3 unsigned short*)(kc+16*beta-2);
            #pragma unroll
            for(int i=0;i<4;i++) d[i]=__builtin_amdgcn_alignbit(e[3-i],e[3-i],16);
            r8= lane==63 ? 0u : pm;
          }
        } else if(chi==1){
          unsigned e=*(AS3 unsigned*)(kc+2*(256+2*beta)); unsigned c2_=*(AS3 unsigned short*)(kc+2*(258+2*beta));
          d[0]=c2_|(c2_<<16); d[1]=__builtin_amdgcn_perm(e,e,0x03020302u); d[2]=__builtin_amdgcn_alignbit(e,e,16); d[3]=__builtin_amdgcn_perm(e,e,0x01000100u);
          r8=e;
        } else {
          unsigned c0=*(AS3 unsigned short*)(kc+2*(384+beta)), c1=*(AS3 unsigned short*)(kc+2*(385+beta));
          d[0]=c1|(c1<<16); d[1]=c1|(((beta&1)?c1:c0)<<16); d[2]=c0|(c0<<16); d[3]=d[2];
          r8=c0;
        }
        u32x4 f;
        f[0]=__builtin_amdgcn_alignbit(d[1],d[0],16); f[1]=__builtin_amdgcn_alignbit(d[2],d[1],16);
        f[2]=__builtin_amdgcn_alignbit(d[3],d[2],16); f[3]=__builtin_amdgcn_alignbit(r8,d[3],16);
        *(AS3 u32x4*)(R+R_P0+lane*16)=d; *(AS3 u32x4*)(R+R_P1+lane*16)=f;
        if(lane<2){ *(AS3 u32x4*)(R+R_P0+1024+lane*16)=sv0; *(AS3 u32x4*)(R+R_P1+1024+lane*16)=sv1; }
        sv0=d; sv1=f;
        u32x4 A[16];
        #pragma unroll
        for(int m=0;m<16;m++){
          AS3 unsigned* p=(AS3 unsigned*)(ak+64*(15-m));
          A[m]=u32x4{p[0],p[1],p[2],p[3]};
        }
        const int ne=8-2*chi;
        bf16x8 Bf[2][8];
        #pragma unroll
        for(int e=0;e<ne;e++) Bf[0][e]=*(AS3 bf16x8*)(bu+448+512*e);
        SB();
        #pragma unroll
        for(int m0=0;m0<8;m0++){
          if(m0<7){
            #pragma unroll
            for(int e=0;e<ne;e++) Bf[(m0+1)&1][e]=*(AS3 bf16x8*)(bu+448+512*e-64*(m0+1));
          }
          #pragma unroll
          for(int e=0;e<ne;e++){
            y[e+2*chi]=MFMA16(BC8(A[m0]),Bf[m0&1][e],y[e+2*chi]);
            if(e+2*chi+1<8) y[e+2*chi+1]=MFMA16(BC8(A[8+m0]),Bf[m0&1][e],y[e+2*chi+1]);
          }
          #pragma unroll
          for(int e=0;e<ne;e++){ SG(0x8,1); SG(0x100,1); }
          SG(0x8,8);
          SB();
        }
      }
    }
    ST(4);
    {
      const float dk=blo(q_dk);
      #pragma unroll
      for(int i=0;i<8;i++){
        u32x2 uu=*(AS3 u32x2*)(R+480+512*i+32*l15+8*g4);
        u32x2 o={pk2(y[i][0]+blo(uu[0])*dk,y[i][1]+bhi(uu[0])*dk),pk2(y[i][2]+blo(uu[1])*dk,y[i][3]+bhi(uu[1])*dk)};
        __builtin_amdgcn_raw_buffer_store_b64(o,rs,32*l15+8*g4+512*i,WS_BT+ch*4096,16);
      }
      WAITV(0);
      if(lds_arrive(lflag+10,lane)==3 && lane<16)
        __hip_atomic_store((gu32*)(ws+WS_FB+bid_g2(lane,c)*128+r*4),tag,RLX_AGENT);
    }
    ST(5);

  } else {
    const int wr=wid>>1, wh=wid&1;
    unsigned epoch=__hip_atomic_load((gu32*)(ws+WS_EPOCH), RLX_AGENT);
    if(wid==0 && lane<16) lflag[lane]=0u;
    unsigned ax[2];
    { int row=64*wr+l15; int f=(row>>1)&7;
      #pragma unroll
      for(int ks=0;ks<2;ks++) ax[ks]=row*128+(((4*ks+g4)^f)*16); }
    unsigned aw[2];
    { int q=l15>>2,p=lane&3; int sw=(((q>>1)&1)|((g4&1)<<1))<<1;
      #pragma unroll
      for(int n=0;n<2;n++){ int cc=(4*wh+2*n+(p>>1))^sw; aw[n]=16384+(8*g4+q)*128+cc*16+8*(p&1);} }
    f32x4 acc[4][6]={};
    struct Fr{ bf16x8 x[4]; bf16x8 w[6]; };
    auto rd=[&](Fr&F,int slot,int ks){
      AS3 char* b=smem+slot*SLOT;
      #pragma unroll
      for(int m=0;m<4;m++) F.x[m]=*(AS3 bf16x8*)(b+ax[ks]+m*2048);
      #pragma unroll
      for(int sg=0;sg<3;sg++)
      #pragma unroll
      for(int n=0;n<2;n++){ AS3 char* bw=b+aw[n]+ks*4096+sg*8192; F.w[sg*2+n]=CAT8(TR16(bw),TR16(bw+512)); }
    };
    auto mm=[&](const Fr&F){
      #pragma unroll
      for(int n=0;n<6;n++)
      #pragma unroll
      for(int m=0;m<4;m++) acc[m][n]= n<2 ? MFMA16(F.w[n],F.x[m],acc[m][n]) : MFMA16(F.x[m],F.w[n],acc[m][n]);
    };
    Fr F0,F1;
    #define I1() SG(0x8,1);SG(0x100,1);
    #define ILV() do{ I1()I1()I1()I1()I1()I1()I1()I1()I1()I1()I1()I1()I1()I1()I1()I1() SG(0x8,8);}while(0)
    const unsigned tag=__builtin_amdgcn_readfirstlane(epoch)+TAGINC;
    __builtin_amdgcn_s_waitcnt(0xC07F);
    BAR_L();
    ST(1);
    rd(F0,0,0);
    int slot=0;
    SB();
    for(int s=0;s<NST-1;s++){
      rd(F1,slot,1); mm(F0); ILV(); SB();
      slot = slot==2?0:slot+1;
      BAR_L(); SB(); rd(F0,slot,0);
      mm(F1); ILV(); SB();
    }
    rd(F1,slot,1); mm(F0); ILV(); SB();
    BAR_L(); SB();
    mm(F1);
    ST(2);
    __builtin_amdgcn_s_setprio(2);
    const u32x4 ones={0x3F803F80u,0x3F803F80u,0x3F803F80u,0x3F803F80u};
    f32x4 kvt[2][2]={}, zt[2]={};
    #pragma unroll
    for(int mu=0;mu<2;mu++){
      u32x4 pv0=pk8(acc[2*mu][4],acc[2*mu+1][4]), pv1=pk8(acc[2*mu][5],acc[2*mu+1][5]);
      #pragma unroll
      for(int dl=0;dl<2;dl++){
        u32x4 pk=pk8(phi4(acc[2*mu][2+dl]),phi4(acc[2*mu+1][2+dl]));
        kvt[dl][0]=MFMA16(BC8(pk),BC8(pv0),kvt[dl][0]);
        kvt[dl][1]=MFMA16(BC8(pk),BC8(pv1),kvt[dl][1]);
        zt[dl]=MFMA16(BC8(pk),BC8(ones),zt[dl]);
      }
    }
    AS3 char* inbox=smem+wid*10240;
    AS3 char* outbox=smem+(wid^2)*10240;
    {
      f32x4 s0=wr?kvt[0][0]:kvt[0][1], s1=wr?kvt[1][0]:kvt[1][1];
      *(AS3 f32x4*)(outbox+lane*16)=s0; *(AS3 f32x4*)(outbox+1024+lane*16)=s1;
      WAITL();
      if(lane==0) __hip_atomic_store(lflag+(wid^2),1u,RLX_WG);
      if(l15==0){
        unsigned zo=WS_ZP+((((c*2+wh)*2+wr)*16+r)*4+g4)*32;
        STB128(__builtin_bit_cast(u32x4,zt[0]),zo,0);
        STB128(__builtin_bit_cast(u32x4,zt[1]),zo+16,0);
      }
      f32x4 o0=wr?kvt[0][1]:kvt[0][0], o1=wr?kvt[1][1]:kvt[1][0];
      lds_wait_ge(lflag+wid,1u);
      o0+=*(AS3 f32x4*)(inbox+lane*16); o1+=*(AS3 f32x4*)(inbox+1024+lane*16);
      STB128(pk8(o0,o1),lane*16,WS_KVP+(((c*2+wh)*2+wr)*16+r)*1024);
    }
    ST(3);
    u32x4 pq[4];
    #pragma unroll
    for(int m=0;m<4;m++) pq[m]=pk8(phi4(acc[m][0]),phi4(acc[m][1]));
    asm volatile("s_waitcnt vmcnt(0)":"+v"(pq[0]),"+v"(pq[1]),"+v"(pq[2]),"+v"(pq[3])::"memory");
    if(lds_arrive(lflag+6,lane)==3){
      if(lane<16) __hip_atomic_store((gu32*)(ws+WS_FKV+bid_g1(lane,c)*128+r*4),tag,RLX_AGENT);
      ST(4);
      poll_all((gu32*)(ws+WS_FKV+bid*128+l15*4),tag);
      if(lane==0) __hip_atomic_store(lflag+7,1u,RLX_WG);
    } else { ST(4); lds_wait_ge(lflag+7,1u); }
    ST(5);
    u32x4 kvF[2], zF;
    {
      u32x4 P[16], Z[4];
      unsigned ko=WS_KVP+((c*2+wh)*2+wr)*16384;
      unsigned zo=WS_ZP+(((c*2+wh)*2)*16+l15)*128+g4*32;
      Z[0]=LDB128(zo,0); Z[1]=LDB128(zo+16,0); Z[2]=LDB128(zo+2048,0); Z[3]=LDB128(zo+2048+16,0);
      #pragma unroll
      for(int p=0;p<16;p++) P[p]=LDB128(lane*16,ko+((p+2*r)&15)*1024);
      SB();
      u32x4 sel0={0,0,0,0}, sel1={0,0,0,0};
      { unsigned on=((l15>>2)==g4)?((l15&1)?0x3F800000u:0x00003F80u):0u;
        unsigned lo=(l15&2)?0u:on, hi=(l15&2)?on:0u;
        sel0[0]=lo; sel0[1]=hi; sel1[2]=lo; sel1[3]=hi; }
      f32x4 z0=__builtin_bit_cast(f32x4,Z[0])+__builtin_bit_cast(f32x4,Z[2]);
      f32x4 z1=__builtin_bit_cast(f32x4,Z[1])+__builtin_bit_cast(f32x4,Z[3]);
      #pragma unroll
      for(int i=0;i<4;i++){ z0[i]=row_sum16(z0[i]); z1[i]=row_sum16(z1[i]); }
      zF=pk8(z0,z1);
      f32x4 d0={},d1={};
      #pragma unroll
      for(int p=0;p<16;p++){ d0=MFMA16(BC8(sel0),BC8(P[p]),d0); d1=MFMA16(BC8(sel1),BC8(P[p]),d1); }
      u32x4 own=pk8(d0,d1);
      *(AS3 u32x4*)(outbox+2048+lane*16)=own;
      WAITL();
      if(lane==0) __hip_atomic_store(lflag+(wid^2),2u,RLX_WG);
      lds_wait_ge(lflag+wid,2u);
      u32x4 oth=*(AS3 u32x4*)(inbox+2048+lane*16);
      kvF[0]=wr?oth:own; kvF[1]=wr?own:oth;
    }
    ST(6);
    #pragma unroll
    for(int m=0;m<4;m++){
      const f32x4 zero={};
      f32x4 den=MFMA16(BC8(zF),BC8(pq[m]),zero);
      f32x4 n0=MFMA16(BC8(kvF[0]),BC8(pq[m]),zero), n1=MFMA16(BC8(kvF[1]),BC8(pq[m]),zero);
      float rc=__builtin_amdgcn_rcpf(den[0]+1e-6f);
      STB128(pk8(n0*rc,n1*rc),lane*16,WS_OF+((8*r+4*wr+m)*32+2*c+wh)*1024);
    }
    ST(7);
    lds_wait_ge(lflag+4,4u);
    WAITV(0);
    const bool lastC = lds_arrive(lflag+8,lane)==3;
    if(lastC && lane<16) __hip_atomic_store((gu32*)(ws+WS_FO+bid_g2(r,lane)*128+c*4),tag,RLX_AGENT);
    ST(8);
    #pragma unroll
    for(int i=0;i<10;i++){ int p=88+10*wid+i; glds16(wob+(size_t)((p+8*c2)&127)*16384,vwo,lbase+(p-88)*1024); }
    unsigned ao[4];
    { int q=l15>>2,p=lane&3; int sw=((2*g4+(q>>1))&3)<<1;
      #pragma unroll
      for(int n=0;n<4;n++) ao[n]=(4*g4+q)*128+(((2*n+(p>>1))^sw)*16)+8*(p&1); }
    if(lastC){
      poll_all((gu32*)(ws+WS_FO+bid*128+l15*4),tag);
      if(lane==0) __hip_atomic_store(lflag+9,1u,RLX_WG);
      if(bid==0 && lane==0) __hip_atomic_store((gu32*)(ws+WS_EPOCH),tag,RLX_AGENT);
    } else lds_wait_ge(lflag+9,1u);
    ST(9);
    f32x4 a2[4][2]={};
    const unsigned oo=WS_OF+(8*r2+2*wid)*32768;
#define PF 6
    u32x4 B[32][2];
    auto ldB=[&](int h){
      B[h][0]=LDB128(lane*16,oo+((h+2*c2)&31)*1024);
      B[h][1]=LDB128(lane*16,oo+32768+((h+2*c2)&31)*1024);
    };
    bf16x8 A[2][4];
    auto rdA=[&](int h){
      const unsigned hb = h<22 ? L_WO+4096*h : 4096*(h-22);
      #pragma unroll
      for(int n=0;n<4;n++){ AS3 char* bw=smem+ao[n]+hb; A[h&1][n]=CAT8(TR16(bw),TR16(bw+2048)); }
    };
    #pragma unroll
    for(int h=0;h<PF;h++) ldB(h);
    rdA(0);
    SB();
    #pragma unroll
    for(int h=0;h<32;h++){
      if(h==1){ lds_arrive(lflag+5,lane); SB(); }
      if(h==21){ lds_wait_ge(lflag+5,4u); SB(); }
      if(h+1<32) rdA(h+1);
      if(h+PF<32) ldB(h+PF);
      #pragma unroll
      for(int n=0;n<4;n++){ a2[n][0]=MFMA16(A[h&1][n],BC8(B[h][0]),a2[n][0]); a2[n][1]=MFMA16(A[h&1][n],BC8(B[h][1]),a2[n][1]); }
      SG(0x8,1);SG(0x100,2);SG(0x8,1);SG(0x100,2);SG(0x8,1);SG(0x100,2);SG(0x8,1);SG(0x100,2);SG(0x8,1);SG(0x20,1);SG(0x8,1);SG(0x20,1);SG(0x8,2);
      SB();
    }
    ST(10);
    poll_all((gu32*)(ws+WS_FB+bid*128+l15*4),tag);
    {
      const int t0=128*r2+32*wid;
      u32x4 bt[4];
      #pragma unroll
      for(int n=0;n<4;n++) bt[n]=LDB128(l15*4096+g4*16,WS_BT+(64*c2+16*n)*4096+t0*2);
      float gv[2];
      #pragma unroll
      for(int mu=0;mu<2;mu++) gv[mu]=__builtin_bit_cast(float,__hip_atomic_load((gu32*)(ws+WS_G+(t0+16*mu+l15)*4),RLX_AGENT));
      #pragma unroll
      for(int mu=0;mu<2;mu++){
        u32x4 sel={0,0,0,0};
        { unsigned on=(g4==2*mu+(l15>>3))?((l15&1)?0x3F800000u:0x00003F80u):0u; int dw=(l15&7)>>1;
          sel[0]=dw==0?on:0u; sel[1]=dw==1?on:0u; sel[2]=dw==2?on:0u; sel[3]=dw==3?on:0u; }
        char* op=out+(size_t)(t0+16*mu+l15)*2048+c2*128+g4*8;
        const float g=gv[mu], h1=rbf(1.f-g);
        #pragma unroll
        for(int n=0;n<4;n++){
          const f32x4 zero={};
          f32x4 bb=MFMA16(BC8(bt[n]),BC8(sel),zero);
          f32x4 o=a2[n][mu]*g+bb*h1;
          unsigned long long v=(unsigned long long)pk2(o[0],o[1])|((unsigned long long)pk2(o[2],o[3])<<32); __hip_atomic_store((AS1 unsigned long long*)(op+n*32),v,RLX_AGENT); }
      }
    }
    WAITV(0);
    ST(11);
    if(bid==DBID && wid==0){ unsigned long long dl=(tr[DB]-tr[DA])*10ull; if(dl>100000ull) dl=100000ull; while(__builtin_amdgcn_s_memrealtime()-tr[11]<dl){ __builtin_amdgcn_s_sleep(2);} }
  }
}
extern "C" void kernel_launch(void* const* d_in, const int* in_sizes, int n_in,
                              void* d_out, int out_size, void* d_ws, size_t ws_size,
                              hipStream_t stream) {
  (void)hipFuncSetAttribute((const void*)fused,hipFuncAttributeMaxDynamicSharedMemorySize,LDS_BYTES);
  fused<<<256,512,LDS_BYTES,stream>>>((const char*)d_in[0],(const char*)d_in[1],(const char*)d_in[2],
     (const char*)d_in[4],(const char*)d_in[5],(const char*)d_in[6],(const char*)d_in[7],
     (const char*)d_in[8],(const char*)d_in[9],(const char*)d_in[10],(char*)d_out,(char*)d_ws);
}
```

```cpp
#include <hip/hip_runtime.h>
#include <hip/hip_bf16.h>
#include <type_traits>
#include <utility>
#define AS1 __attribute__((address_space(1)))
#define AS3 __attribute__((address_space(3)))
#define DI __device__ __forceinline__
#define AI __attribute__((always_inline))
typedef __bf16 bf16x2 __attribute__((ext_vector_type(2)));
typedef __bf16 bf16x4 __attribute__((ext_vector_type(4)));
typedef __bf16 bf16x8 __attribute__((ext_vector_type(8)));
typedef float f32x2 __attribute__((ext_vector_type(2)));
typedef float f32x4 __attribute__((ext_vector_type(4)));
typedef float f32x16 __attribute__((ext_vector_type(16)));
typedef unsigned u32x2 __attribute__((ext_vector_type(2)));
typedef unsigned u32x4 __attribute__((ext_vector_type(4)));
typedef AS1 unsigned gu32;

constexpr int SLOT=40960, NST=16, LDS_BYTES=163840;
constexpr unsigned TAGINC=0x9E3779B9u;
constexpr unsigned SPINCAP=1u<<18;
constexpr unsigned WS_EPOCH=0;
constexpr unsigned WS_G  =4096;
constexpr unsigned WS_FKV=65536;
constexpr unsigned WS_FO =WS_FKV+32768;
constexpr unsigned WS_FX =WS_FO+32768;
constexpr unsigned WS_FB =WS_FX+32768;
constexpr unsigned WS_ZP =262144;
constexpr unsigned WS_KVP=1u<<20;
constexpr unsigned WS_OF =2u<<20;
constexpr unsigned WS_XT =6u<<20;
constexpr unsigned WS_BT =10u<<20;
constexpr unsigned WS_END=14u<<20;
constexpr unsigned WS_DBG=16u<<20;
constexpr unsigned L_E=122880, L_GW=159744, L_FLAG=161792;
constexpr unsigned R_KC=4576, R_WO=5888, R_IN=9216, Q_P1=4160;
#define SPS 5
#define LF(i) ((AS3 unsigned*)(smem+L_FLAG+(i)*4))

DI void glds16(const void* gbase, unsigned voff, unsigned lds){
  asm volatile("s_mov_b32 m0, %2\n\ts_nop 0\n\tglobal_load_lds_dwordx4 %0, %1"
     :: "v"(voff), "s"(gbase), "s"(lds) : "memory");
}
#define WAITV(n) asm volatile("s_waitcnt vmcnt(" #n ")":::"memory")
#define WAITL() asm volatile("s_waitcnt lgkmcnt(0)":::"memory")
#define BAR_L() asm volatile("s_waitcnt lgkmcnt(0)\n\ts_barrier":::"memory")
#define MFMA16(a,b,c) __builtin_amdgcn_mfma_f32_16x16x32_bf16((a),(b),(c),0,0,0)
#define MFMA32(a,b,c) __builtin_amdgcn_mfma_f32_32x32x16_bf16((a),(b),(c),0,0,0)
#define SB() __builtin_amdgcn_sched_barrier(0)
#define SG(m,n) __builtin_amdgcn_sched_group_barrier(m,n,0)
#define RLX_AGENT __ATOMIC_RELAXED, __HIP_MEMORY_SCOPE_AGENT
#define RLX_WG __ATOMIC_RELAXED, __HIP_MEMORY_SCOPE_WORKGROUP
#define BC8(x) __builtin_bit_cast(bf16x8,(x))
#define TR16(p) __builtin_amdgcn_ds_read_tr16_b64_v4bf16((AS3 bf16x4*)(p))
#define CAT8(a,b) __builtin_shufflevector((a),(b),0,1,2,3,4,5,6,7)
#define LDB128(off,soff) __builtin_amdgcn_raw_buffer_load_b128(rs,(off),(soff),16)
#define STB128(v,off,soff) __builtin_amdgcn_raw_buffer_store_b128((v),rs,(off),(soff),16)
#define PIN(x) asm volatile("":"+v"(x))

#define ST(i)
constexpr float L2E=1.44269504088896f;
constexpr float EPS=1e-6f, ACC0=1.f+1e-6f;

DI unsigned pk2(float a,float b){ f32x2 t={a,b}; bf16x2 p=__builtin_convertvector(t,bf16x2); return __builtin_bit_cast(unsigned,p);}
DI unsigned pk2s(float a,float b){ asm("":"+v"(a)); asm("":"+v"(b)); return pk2(a,b); }
DI u32x4 pk8(const f32x4&a,const f32x4&b){ u32x4 r; r[0]=pk2(a[0],a[1]); r[1]=pk2(a[2],a[3]); r[2]=pk2(b[0],b[1]); r[3]=pk2(b[2],b[3]); return r; }
DI float u2f(unsigned u){ return __builtin_bit_cast(float,u); }
DI float blo(unsigned w){ return u2f(w<<16); }
DI float bhi(unsigned w){ return u2f(w&0xffff0000u); }
DI float rbf(float v){ return blo(pk2(v,v)); }
DI float phi1(float a){
  float e=__builtin_amdgcn_fmed3f(__builtin_amdgcn_exp2f(__builtin_fmaf(a,L2E,-L2E*ACC0)),0.f,1.f)+EPS;
  asm("":"+v"(e));
  int x=__builtin_bit_cast(int,a), y=__builtin_bit_cast(int,e); return __builtin_bit_cast(float,x>y?x:y); }
DI u32x4 phi8(const f32x4&a,const f32x4&b){ u32x4 r; r[0]=pk2(phi1(a[0]),phi1(a[1])); r[1]=pk2(phi1(a[2]),phi1(a[3])); r[2]=pk2(phi1(b[0]),phi1(b[1])); r[3]=pk2(phi1(b[2]),phi1(b[3])); return r; }
DI void poll_all(const gu32* p, unsigned tag){
  #pragma unroll 1
  for(unsigned it=0; it<SPINCAP; ++it){
    unsigned v=__hip_atomic_load(p, RLX_AGENT);
    if(__builtin_amdgcn_ballot_w64(v!=tag)==0ull) break;
    __builtin_amdgcn_s_sleep(1);
  }
  asm volatile("":::"memory");
}
template<int SL> DI void lds_wait_ge(AS3 unsigned* p, unsigned want){
  #pragma unroll 1
  for(unsigned it=0; it<(SPINCAP<<4); ++it){
    unsigned v=__hip_atomic_load(p,RLX_WG);
    if((unsigned)__builtin_amdgcn_readfirstlane(v)>=want) break;
    if(SL) __builtin_amdgcn_s_sleep(SL);
  }
  asm volatile("":::"memory");
}
DI unsigned lds_arrive(AS3 unsigned* p,int lane){
  unsigned old=0; if(lane==0) old=__hip_atomic_fetch_add(p,1u,RLX_WG);
  return __builtin_amdgcn_readfirstlane(old);
}
template<int CTRL> DI float dpp_add(float x){
  int y=__builtin_amdgcn_update_dpp(0,__builtin_bit_cast(int,x),CTRL,0xf,0xf,true);
  return x+__builtin_bit_cast(float,y);
}
DI float row_sum16(float x){ return dpp_add<0x121>(dpp_add<0x122>(dpp_add<0x124>(dpp_add<0x128>(x)))); }
template<int N> using IC=std::integral_constant<int,N>;
template<class F,int...I> DI void sfor_(F&&f,std::integer_sequence<int,I...>){ (f(IC<I>{}),...); }
template<int N,class F> DI void sfor(F&&f){ sfor_(f,std::make_integer_sequence<int,N>{}); }
struct Cx{ float r0,i0,r1,i1; };
#define PINC(v) asm volatile("":"+v"(v.r0),"+v"(v.i0),"+v"(v.r1),"+v"(v.i1))
DI Cx cmul(const Cx&a,const Cx&b){ Cx r;
  r.r0=__builtin_fmaf(a.r0,b.r0,-(a.i0*b.i0)); r.i0=__builtin_fmaf(a.r0,b.i0,a.i0*b.r0);
  r.r1=__builtin_fmaf(a.r1,b.r1,-(a.i1*b.i1)); r.i1=__builtin_fmaf(a.r1,b.i1,a.i1*b.r1);
  PINC(r); return r; }
DI void cexp1(float lr,float rev,float p,float&re,float&im){
  float e=__builtin_amdgcn_exp2f(lr*p); float f=__builtin_amdgcn_fractf(rev*p);
  re=e*__builtin_amdgcn_cosf(f); im=e*__builtin_amdgcn_sinf(f); asm volatile("":"+v"(re),"+v"(im));
}
DI int bid_g1(int r,int c){ return (((r&7)|((c&3)<<3))<<3)|(r>>3)|((c>>2)<<1); }
#define MAP2 1
DI int bid_g2(int r2,int c2){ return ((((r2&3)<<3)|(c2&7))<<3)|((r2>>2)<<1)|(c2>>3); }

__global__ __launch_bounds__(512,2) void fused(const char* __restrict__ x, const char* __restrict__ w, const char* __restrict__ wo,
     const char* __restrict__ pla, const char* __restrict__ pai, const char* __restrict__ pcr, const char* __restrict__ pci,
     const char* __restrict__ pdt, const char* __restrict__ pdk, const char* __restrict__ pgw,
     char* __restrict__ out, char* ws){
  extern __shared__ __attribute__((aligned(16))) char smem_[];
  AS3 char* smem=(AS3 char*)smem_;
  const int tid=threadIdx.x, lane=tid&63;
  const int wid=__builtin_amdgcn_readfirstlane(tid>>6);
  const int bid=blockIdx.x, g8=bid&7, idx=bid>>3;
  const int r=(g8&1)*8+(idx&7), c=(g8>>1)*4+(idx>>3);
  const int r2=4*(g8>>1)+(idx>>3), c2=8*(g8&1)+(idx&7), hp=(2*(idx&7)+4*(idx>>3))&15;
  const int g4=lane>>4, l15=lane&15;
#define QX 1
  const int cq=c^QX;
  ST(0);
  asm volatile(""::"s"(x),"s"(w),"s"(wo),"s"(pla),"s"(pai),"s"(pcr),"s"(pci),"s"(pdt),"s"(pdk),"s"(pgw),"s"(out),"s"(ws));
  const unsigned lbase=(unsigned)(size_t)smem;
  const __amdgpu_buffer_rsrc_t rs=__builtin_amdgcn_make_buffer_rsrc(ws,0,WS_END,0x00020000);
  if(__builtin_expect(wid>=4,1)){
    const int lw=wid-4;
    const int ch=64*c+4*r+lw;
    AS3 char* R=smem+2*SLOT+lw*10240;
    AS3 char* Q=smem+lw*10240;
    const int xb8=(4*(g8>>1)+8*(g8&1))&15, rho=(r+c)&1, rho4=(r+c)&3;
    unsigned vx[4], vw[6];
    #pragma unroll
    for(int i=0;i<4;i++){ int row=8*(4*lw+i)+(lane>>3); int cc=(lane&7)^((row>>1)&7); vx[i]=row*2048+cc*16; }
    #pragma unroll
    for(int i=0;i<6;i++){ int p=6*lw+i; int seg=p>>3, kr=(p&7)*8+(lane>>3); int cc=(lane&7)^((((kr>>1)&1)|(((kr>>3)&1)<<1))<<1); vw[i]=kr*6144+seg*2048+cc*16+(seg==0?cq:c)*128; }
    const char* xb=x+(size_t)r*128*2048;
    const char* wb=w;
    #define GX(i) glds16(xs,vx[i],ls+(4*lw+i)*1024)
    #define GW(i) glds16(wsb,vw[i],ls+16384+(6*lw+i)*1024)
#define MIX 16
    #define KC(s) ((xb8+((s)<MIX ? ((s)^rho) : ((s)^rho4)))&15)
    #define ISSUE_SETUP(s) const unsigned ls=lbase+((s)%3)*SLOT; const int kc=KC(s); const char* xs=xb+kc*128; const char* wsb=wb+(size_t)kc*64*6144;
    auto issue=[&](int s,int) AI { ISSUE_SETUP(s) GW(0);GX(0);GW(1);GW(2);GX(1);GW(3);GX(2);GW(4);GW(5);GX(3); };
    issue(0,0);
    ST(6);
    unsigned q_la,q_ai,q_cr,q_ci,q_dt,q_dk,q_ep;
    { unsigned o=ch*256+lane*4, o2=ch*2, z=0;
      asm volatile("global_load_dword %0, %1, %2":"=v"(q_la):"v"(o),"s"(pla):"memory");
      asm volatile("global_load_dword %0, %1, %2":"=v"(q_ai):"v"(o),"s"(pai):"memory");
      asm volatile("global_load_dword %0, %1, %2":"=v"(q_cr):"v"(o),"s"(pcr):"memory");
      asm volatile("global_load_dword %0, %1, %2":"=v"(q_ci):"v"(o),"s"(pci):"memory");
      asm volatile("global_load_ushort %0, %1, %2":"=v"(q_dt):"v"(o2),"s"(pdt):"memory");
      asm volatile("global_load_ushort %0, %1, %2":"=v"(q_dk):"v"(o2),"s"(pdk):"memory");
      asm volatile("global_load_dword %0, %1, %2 sc1":"=v"(q_ep):"v"(z),"s"(ws):"memory");
      if(lw==3){ glds16(pgw,lane*16,lbase+L_GW); glds16(pgw+1024,lane*16,lbase+L_GW+1024); } }
    const int xchunk=xb8+(c&3), xstage=(c&3)^rho;
    bf16x8 XT[4]; float gs=0.f;
    const unsigned gxo=(8*c+2*lw+(lane>>5))*128+((((lane&31)>>2)^((4*c+lw)&7))*16)+(lane&3)*4;
    auto gate=[&](int s,int slot) AI {
      const int kc=KC(s);
      unsigned xa=*(AS3 unsigned*)(smem+slot*SLOT+gxo), ga=*(AS3 unsigned*)(smem+L_GW+kc*128+(lane&31)*4);
      gs=__builtin_fmaf(blo(xa),blo(ga),gs); gs=__builtin_fmaf(bhi(xa),bhi(ga),gs); PIN(gs);
    };
    issue(1,1);
    asm volatile("s_waitcnt vmcnt(10)\n\ts_barrier":"+v"(q_la),"+v"(q_ai),"+v"(q_cr),"+v"(q_ci),"+v"(q_dt),"+v"(q_dk),"+v"(q_ep)::"memory");
    const unsigned tag=__builtin_amdgcn_readfirstlane(q_ep)+TAGINC;
    u32x2 TB[57]; Cx w1,w2,w4,w8c,w128,G,E; float lr0,lr1,rv0,rv1;
    auto put=[&](int e,const Cx&v) AI { TB[e]=u32x2{pk2(v.r0,v.i0),pk2(v.r1,v.i1)}; PIN(TB[e]); };
    auto step=[&](auto KK) AI { constexpr int k=decltype(KK)::value;
      if constexpr(k==0){ const float dt=rbf(__builtin_amdgcn_exp2f(blo(q_dt)*L2E));
        lr0=-rbf(__builtin_amdgcn_exp2f(blo(q_la)*L2E))*(dt*L2E); PIN(lr0);
        lr1=-rbf(__builtin_amdgcn_exp2f(bhi(q_la)*L2E))*(dt*L2E); PIN(lr1);
        rv0=dt; PIN(rv0); }
      else if constexpr(k==1){ const float dt=rv0;
        rv0=blo(q_ai)*(dt*0.159154943091895f); PIN(rv0); rv1=bhi(q_ai)*(dt*0.159154943091895f); PIN(rv1);
        G.r0=blo(q_cr)*(2.f*dt); G.i0=blo(q_ci)*(2.f*dt); G.r1=bhi(q_cr)*(2.f*dt); G.i1=bhi(q_ci)*(2.f*dt); PINC(G); }
      else if constexpr(k==2){ cexp1(lr0,-rv0,8.f,w8c.r0,w8c.i0); }
      else if constexpr(k==3){ cexp1(lr1,-rv1,8.f,w8c.r1,w8c.i1); }
      else if constexpr(k==4){ cexp1(lr0,rv0,1.f,w1.r0,w1.i0); }
      else if constexpr(k==5){ cexp1(lr1,rv1,1.f,w1.r1,w1.i1); }
      else if constexpr(k==6){ cexp1(lr0,rv0,128.f,w128.r0,w128.i0); }
      else if constexpr(k==7){ cexp1(lr1,rv1,128.f,w128.r1,w128.i1); }
      else if constexpr(k==8){ w2=cmul(w1,w1); }
      else if constexpr(k==9){ w4=cmul(w2,w2); }
      else if constexpr(k==10){ TB[0]=u32x2{0x3F80u,0x3F80u}; E=w8c; put(1,E); }
      else if constexpr(k>=11 && k<25){ E=cmul(E,w8c); put(k-9,E); }
      else if constexpr(k>=25 && k<41){ constexpr int e=k-25; if constexpr((e&7)==0){ E=G; G=cmul(G,w128); } else E=cmul(E,w1); put(16+e,E); }
      else if constexpr(k>=41 && k<49){ constexpr int e=k-41; if constexpr((e&3)==0){ E=G; G=cmul(G,w128); } else E=cmul(E,w2); put(32+e,E); }
      else if constexpr(k>=49 && k<57){ constexpr int e=k-41; if constexpr((e&1)==0){ E=G; G=cmul(G,w128); } else E=cmul(E,w4); put(32+e,E); }
      else if constexpr(k>=57 && k<66){ put(48+k-57,G); if constexpr(k<65) G=cmul(G,w128); }
    };
#define S0 1
    #define STEP(s,j) do{ if constexpr((j)<SPS && (s)>=S0) step(IC<SPS*((s)-S0)+(j)>{}); }while(0)
    sfor<NST-2>([&](auto SS) AI { constexpr int s=decltype(SS)::value;
      { ISSUE_SETUP(s+2)
        GW(0);GX(0);GW(1);GW(2);GX(1);GW(3);STEP(s,0);GX(2);GW(4);STEP(s,1);GW(5);GX(3);STEP(s,2);STEP(s,3);
      }
      STEP(s,4);STEP(s,5);
      if(s<4 && s==xstage){
        int q=l15>>2,p=lane&3;
        AS3 char* b=smem+(s%3)*SLOT+(8*g4+q)*128+8*(p&1);
        AS3 char* b0=b+(((2*lw+(p>>1))^((4*g4+(q>>1))&7))*16);
        AS3 char* b1=b+512+(((2*lw+(p>>1))^((4*g4+2+(q>>1))&7))*16);
        #pragma unroll
        for(int j=0;j<4;j++) XT[j]=CAT8(TR16(b0+j*4096),TR16(b1+j*4096));
      }
      gate(s,s%3);
      asm volatile("s_waitcnt vmcnt(10) lgkmcnt(0)\n\ts_barrier":::"memory");
    });
    ST(1);
    const unsigned vwo=(lane>>3)*2048+(((lane&7)^(((lane>>4)&1)<<2))*16);
    const char* wob=wo+c2*128;
    u32x4 WC[10];
    #define GC(i) asm volatile("global_load_dwordx4 %0, %1, %2":"=v"(WC[i]):"v"(vwo),"s"(wob+(size_t)((76+10*lw+(i)+8*hp)&127)*16384):"memory")
    #define GO(p,dst) glds16(wob+(size_t)(((p)+8*hp)&127)*16384,vwo,lbase+(dst))
    #define GE(i) GO(9*lw+(i),L_E+(9*lw+(i))*1024)
    #define G1(i) GO(36+10*lw+(i),SLOT+(10*lw+(i))*1024)
    {
      AS3 char* kgw=R+lane*8;
      AS3 char* kgr=R+l15*528+g4*16;
      bf16x8 OF[8],TF[8],T2[8]; f32x4 kt0={},kt1={},kt2={};
      { unsigned o=WS_XT+((4*xchunk+lw)*16+r)*4096+l15*64+g4*16;
        #pragma unroll
        for(int j=0;j<4;j++) STB128(__builtin_bit_cast(u32x4,XT[j]),o+j*1024,0); }
      GE(0);GE(1);GE(2);GE(3);GE(4);
      STEP(14,0);STEP(14,1);STEP(14,2);STEP(14,3);STEP(14,4);STEP(14,5);
      gate(14,2);
      asm volatile("s_waitcnt vmcnt(9) lgkmcnt(0)\n\ts_barrier":::"memory");
      ST(2);
      GE(5);GE(6);GE(7);GE(8);
      gate(15,0);
      if(lane==0) __hip_atomic_fetch_add(LF(13),1u,RLX_WG);
      sfor<66>([&](auto KK) AI { constexpr int k=decltype(KK)::value; if constexpr(k>=SPS*(15-S0)) step(KK); });
      #define W4(b,e) do{ *(AS3 u32x2*)(kgw+(e)*528)=TB[(b)+(e)]; *(AS3 u32x2*)(kgw+((e)+1)*528)=TB[(b)+(e)+1]; *(AS3 u32x2*)(kgw+((e)+2)*528)=TB[(b)+(e)+2]; *(AS3 u32x2*)(kgw+((e)+3)*528)=TB[(b)+(e)+3]; }while(0)
      W4(0,0); G1(0);G1(1); W4(0,4); G1(2);G1(3); W4(0,8); G1(4);G1(5); W4(0,12);
      WAITV(15);
      if(lane<4) __hip_atomic_store((gu32*)(ws+WS_FX+bid_g1(4*lw+lane,xchunk)*128+r*4),tag,RLX_AGENT);
      #pragma unroll
      for(int t=0;t<8;t++) OF[t]=*(AS3 bf16x8*)(kgr+t*64);
      G1(6);G1(7); W4(16,0); G1(8);G1(9); W4(16,4); GC(0);GC(1); W4(16,8); GC(2);GC(3); W4(16,12);
      #pragma unroll
      for(int t=0;t<8;t++) TF[t]=*(AS3 bf16x8*)(kgr+t*64);
      {
        gs=row_sum16(gs); gs+=u2f(__builtin_amdgcn_permlane16_swap(__builtin_bit_cast(unsigned,gs),__builtin_bit_cast(unsigned,gs),false,false)[1]);
        float g=rbf(__builtin_amdgcn_rcpf(1.f+__builtin_amdgcn_exp2f(-rbf(gs)*L2E)));
        if((lane&31)==0) __hip_atomic_store((gu32*)(ws+WS_G+(128*r+8*c+2*lw+(lane>>5))*4),__builtin_bit_cast(unsigned,g),RLX_AGENT);
      }
      GC(4);GC(5); W4(32,0); GC(6);GC(7); W4(32,4); GC(8);GC(9); W4(32,8);
      unsigned fx; { unsigned fo=WS_FX+bid*128+l15*4; asm volatile("global_load_dword %0, %1, %2 sc1":"=v"(fx):"v"(fo),"s"(ws):"memory"); }
      W4(32,12);
      #pragma unroll
      for(int t=0;t<8;t++) kt0=MFMA16(TF[t],OF[t],kt0);
      #pragma unroll
      for(int t=0;t<8;t++) TF[t]=*(AS3 bf16x8*)(kgr+t*64);
      W4(48,0); W4(48,4); *(AS3 u32x2*)(kgw+8*528)=TB[56];
      #pragma unroll
      for(int t=0;t<8;t++) T2[t]=*(AS3 bf16x8*)(kgr+t*64);
      ST(8);
      asm volatile("s_waitcnt vmcnt(0) lgkmcnt(0)":"+v"(fx)::"memory");
      asm volatile("":"+v"(WC[0]),"+v"(WC[1]),"+v"(WC[2]),"+v"(WC[3]),"+v"(WC[4]),"+v"(WC[5]),"+v"(WC[6]),"+v"(WC[7]),"+v"(WC[8]),"+v"(WC[9]));
      if(__builtin_amdgcn_ballot_w64(fx!=tag)!=0ull) poll_all((gu32*)(ws+WS_FX+bid*128+l15*4),tag);
      { const char* ub=ws+WS_XT+(size_t)(ch>>4)*65536+(ch&15)*64; const unsigned o=(lane>>2)*1024+(lane&3)*16;
        #pragma unroll
        for(int i=0;i<4;i++) asm volatile("s_mov_b32 m0, %2\n\ts_nop 0\n\tglobal_load_lds_dwordx4 %0, %1 sc1"::"v"(o),"s"(ub+i*16384),"s"(lbase+2*SLOT+lw*10240+480+i*1024):"memory");
        #pragma unroll
        for(int i=0;i<3;i++) GO(116+4*i+lw,2*SLOT+lw*10240+R_WO+i*1024); }
      #pragma unroll
      for(int t=0;t<8;t++){ kt1=MFMA16(TF[t],OF[t],kt1); kt2=MFMA16(OF[t],T2[t],kt2); }
      AS3 char* kc=R+R_KC;
      u32x2 t0={pk2(kt0[0],kt0[1]),pk2(kt0[2],kt0[3])};
      *(AS3 u32x2*)(kc+2*(128*(g4>>1)+8*l15+4*(g4&1)))=t0;
      int a0= g4<2 ? 256+64*g4+4*l15 : 384+64*(g4-2)+2*l15;
      int a1= g4<2 ? a0+2 : a0+32;
      *(AS3 unsigned*)(kc+2*a0)=pk2(kt1[0],kt1[1]);
      *(AS3 unsigned*)(kc+2*a1)=pk2(kt1[2],kt1[3]);
      if(l15<9){ u32x2 t2={pk2(kt2[0],kt2[1]),pk2(kt2[2],kt2[3])}; *(AS3 u32x2*)(kc+2*(512+16*l15+4*g4))=t2; }
    }
    ST(7);
    lds_wait_ge<0>(LF(13),8u);
    {
      AS3 char* kc=R+R_KC;
      const int be=63-lane;
      const int b1=be|32, b0=be&31;
      u32x2 e0=*(AS3 u32x2*)(kc+2*(128+4*b1)); unsigned c4=*(AS3 unsigned short*)(kc+2*(132+4*b1));
      u32x4 f0=*(AS3 u32x4*)(kc+16*b0); unsigned pm=*(AS3 unsigned short*)(kc+16*(lane<31?32:be)-2);
      unsigned e1=*(AS3 unsigned*)(kc+2*(384+2*be)); unsigned c2_=*(AS3 unsigned short*)(kc+2*(386+2*be));
      unsigned c20=*(AS3 unsigned short*)(kc+2*(512+be)), c21=*(AS3 unsigned short*)(kc+2*(513+be));
      unsigned c30=*(AS3 unsigned short*)(kc+2*(576+be)), c31=*(AS3 unsigned short*)(kc+2*(577+be));
      { u32x4 z={0,0,0,0};
        if(lane<30) *(AS3 u32x4*)(R+lane*16)=z;
        if(lane<2){ *(AS3 u32x4*)(Q+4096+lane*16)=z; *(AS3 u32x4*)(Q+Q_P1+4096+lane*16)=z; } }
      #pragma unroll
      for(int chi=0;chi<4;chi++){
        u32x4 d; unsigned r8;
        if(chi==0){
          const bool hi=lane<32;
          d[0]=hi?((e0[1]&0xffff0000u)|c4):__builtin_amdgcn_alignbit(f0[3],f0[3],16);
          d[1]=hi?__builtin_amdgcn_perm(e0[1],e0[1],0x01000100u):__builtin_amdgcn_alignbit(f0[2],f0[2],16);
          d[2]=hi?((e0[0]&0xffff0000u)|(e0[1]&0xffffu)):__builtin_amdgcn_alignbit(f0[1],f0[1],16);
          d[3]=hi?__builtin_amdgcn_perm(e0[0],e0[0],0x01000100u):__builtin_amdgcn_alignbit(f0[0],f0[0],16);
          r8= lane<31 ? e0[0] : lane==63 ? 0u : pm;
        } else if(chi==1){
          d[0]=c2_|(c2_<<16); d[1]=__builtin_amdgcn_perm(e1,e1,0x03020302u); d[2]=__builtin_amdgcn_alignbit(e1,e1,16); d[3]=__builtin_amdgcn_perm(e1,e1,0x01000100u);
          r8=e1;
        } else {
          unsigned c0=chi==2?c20:c30, c1=chi==2?c21:c31;
          d[0]=c1|(c1<<16); d[1]=c1|(((be&1)?c1:c0)<<16); d[2]=c0|(c0<<16); d[3]=d[2];
          r8=c0;
        }
        u32x4 f;
        f[0]=__builtin_amdgcn_alignbit(d[1],d[0],16); f[1]=__builtin_amdgcn_alignbit(d[2],d[1],16);
        f[2]=__builtin_amdgcn_alignbit(d[3],d[2],16); f[3]=__builtin_amdgcn_alignbit(r8,d[3],16);
        *(AS3 u32x4*)(Q+(3-chi)*1024+lane*16)=d; *(AS3 u32x4*)(Q+Q_P1+(3-chi)*1024+lane*16)=f;
      }
    }
    ST(9);
    WAITV(0);
    if(lane==0) __hip_atomic_fetch_add(LF(4),1u,RLX_WG);
    __builtin_amdgcn_s_setprio(1);
    f32x4 y[8]={};
    {
      const int n0=15-l15+8*g4;
      AS3 char* ak=Q+((n0&1)?Q_P1:0)+4*(n0>>1)+448;
      AS3 char* bu=R+32*l15+16*g4+448;
      u32x4 A0[8],A1[8]; bf16x8 B0[8],B1[8];
      auto ld=[&](u32x4* A,bf16x8* B,int off) AI {
        #pragma unroll
        for(int k=0;k<8;k++){ AS3 unsigned* p=(AS3 unsigned*)(ak+512*(7-k)-off); A[k]=u32x4{p[0],p[1],p[2],p[3]}; B[k]=*(AS3 bf16x8*)(bu+512*k-off); } };
      auto mmc=[&](const u32x4* A,const bf16x8* B) AI {
        #pragma unroll
        for(int k=0;k<8;k++)
        #pragma unroll
        for(int e=0;e+k<8;e++) y[e+k]=MFMA16(BC8(A[k]),B[e],y[e+k]); };
      #define C1() SG(0x8,1);SG(0x100,1);
      #define CILV() do{ C1()C1()C1()C1()C1()C1()C1()C1()C1()C1()C1()C1()C1()C1()C1()C1()C1()C1()C1()C1()C1()C1()C1()C1() SG(0x8,12);}while(0)
      ld(A0,B0,0);
      ST(3);
      SB();
      #pragma unroll 1
      for(int it=0;it<4;it++){
        ld(A1,B1,64); mmc(A0,B0); CILV(); SB();
        ak-=128; bu-=128;
        if(it==3){ ak+=64; bu+=64; }
        ld(A0,B0,0); mmc(A1,B1); CILV(); SB();
      }
    }
    ST(4);
    {
      const float dk=blo(q_dk);
      #pragma unroll
      for(int i=0;i<8;i++){
        u32x2 uu=*(AS3 u32x2*)(R+480+512*i+32*l15+8*g4);
        u32x2 o={pk2s(y[i][0]+blo(uu[0])*dk,y[i][1]+bhi(uu[0])*dk),pk2s(y[i][2]+blo(uu[1])*dk,y[i][3]+bhi(uu[1])*dk)};
        __builtin_amdgcn_raw_buffer_store_b64(o,rs,32*l15+8*g4+512*i,WS_BT+ch*4096,16);
      }
      #pragma unroll
      for(int i=0;i<10;i++) *(AS3 u32x4*)(Q+lane*16+i*1024)=WC[i];
      asm volatile("":::"memory");
      if(lane==0) __hip_atomic_fetch_add(LF(11),1u,RLX_WG);
      WAITV(0);
      if(lds_arrive(LF(10),lane)==3 && lane<16)
        __hip_atomic_store((gu32*)(ws+WS_FB+bid_g2(lane,c)*128+r*4),tag,RLX_AGENT);
    }
    ST(5);
  } else {
    const int wr=wid>>1, wh=wid&1;
    unsigned epoch=__hip_atomic_load((gu32*)(ws+WS_EPOCH), RLX_AGENT);
    if(wid==0 && lane<16) *LF(lane)=0u;
    unsigned ax[2];
    { int row=64*wr+l15; int f=(row>>1)&7;
      #pragma unroll
      for(int ks=0;ks<2;ks++) ax[ks]=row*128+(((4*ks+g4)^f)*16); }
    unsigned aw[2];
    { int q=l15>>2,p=lane&3; int sw=(((q>>1)&1)|((g4&1)<<1))<<1;
      #pragma unroll
      for(int n=0;n<2;n++){ int cc=(4*wh+2*n+(p>>1))^sw; aw[n]=16384+(8*g4+q)*128+cc*16+8*(p&1);} }
    f32x4 acc[4][6];
    #pragma unroll
    for(int m=0;m<4;m++)
    #pragma unroll
    for(int n=0;n<6;n++){ const float v=n<4?ACC0:0.f; acc[m][n]=f32x4{v,v,v,v}; }
    struct Fr{ bf16x8 x[4]; bf16x8 w[6]; };
    auto rd=[&](Fr&F,int slot,int ks) AI {
      AS3 char* b=smem+slot*SLOT;
      #pragma unroll
      for(int m=0;m<4;m++) F.x[m]=*(AS3 bf16x8*)(b+ax[ks]+m*2048);
      #pragma unroll
      for(int sg=0;sg<3;sg++)
      #pragma unroll
      for(int n=0;n<2;n++){ AS3 char* bw=b+aw[n]+ks*4096+sg*8192; F.w[sg*2+n]=CAT8(TR16(bw),TR16(bw+512)); }
    };
    auto mmn=[&](const Fr&F,int n) AI {
      #pragma unroll
      for(int m=0;m<4;m++) acc[m][n]= n<2 ? MFMA16(F.w[n],F.x[m],acc[m][n]) : MFMA16(F.x[m],F.w[n],acc[m][n]);
    };
    auto mm=[&](const Fr&F) AI {
      #pragma unroll
      for(int n=0;n<6;n++) mmn(F,n);
    };
    Fr F0,F1;
    #define I1() SG(0x8,1);SG(0x100,1);
    #define ILV() do{ I1()I1()I1()I1()I1()I1()I1()I1()I1()I1()I1()I1()I1()I1()I1()I1() SG(0x8,8);}while(0)
    const unsigned tag=__builtin_amdgcn_readfirstlane(epoch)+TAGINC;
    __builtin_amdgcn_s_waitcnt(0xC07F);
    BAR_L();
    ST(1);
    rd(F0,0,0);
    int slot=0;
    SB();
    for(int s=0;s<NST-1;s++){
      rd(F1,slot,1); mm(F0); ILV(); SB();
      slot = slot==2?0:slot+1;
      BAR_L(); SB(); rd(F0,slot,0);
      mm(F1); ILV(); SB();
    }
    rd(F1,slot,1); mm(F0); ILV(); SB();
    WAITL();
    if(lane==0) __hip_atomic_fetch_add(LF(13),1u,RLX_WG);
    ST(2);
    __builtin_amdgcn_s_setprio(2);
    SB();
    mmn(F1,2); mmn(F1,3); mmn(F1,4); mmn(F1,5); mmn(F1,0); mmn(F1,1);
    u32x4 pk[2][2], pv[2][2];
    #pragma unroll
    for(int dl=0;dl<2;dl++)
    #pragma unroll
    for(int mu=0;mu<2;mu++) pk[mu][dl]=phi8(acc[2*mu][2+dl],acc[2*mu+1][2+dl]);
    #pragma unroll
    for(int ep=0;ep<2;ep++)
    #pragma unroll
    for(int mu=0;mu<2;mu++) pv[mu][ep]=pk8(acc[2*mu][4+ep],acc[2*mu+1][4+ep]);
    SG(0x8,6);
    #define V1() SG(0x8,1);SG(0x2,3);
    V1()V1()V1()V1()V1()V1()V1()V1()V1()V1()V1()V1()V1()V1()V1()V1()V1()V1()
    SB();
    const u32x4 ones={0x3F803F80u,0x3F803F80u,0x3F803F80u,0x3F803F80u};
    AS3 char* inbox=smem+2*SLOT+wid*10240+R_IN+lane*16;
    AS3 char* outbox=smem+2*SLOT+(wid^2)*10240+R_IN+lane*16;
    {
      const u32x4 vf0=wr?pv[0][0]:pv[0][1], vf1=wr?pv[1][0]:pv[1][1], vn0=wr?pv[0][1]:pv[0][0], vn1=wr?pv[1][1]:pv[1][0];
      const f32x4 zero={};
      f32x4 s0=MFMA16(BC8(pk[0][0]),BC8(vf0),zero), s1=MFMA16(BC8(pk[0][1]),BC8(vf0),zero);
      s0=MFMA16(BC8(pk[1][0]),BC8(vf1),s0); s1=MFMA16(BC8(pk[1][1]),BC8(vf1),s1);
      f32x4 z0=MFMA16(BC8(pk[0][0]),BC8(ones),zero), z1=MFMA16(BC8(pk[0][1]),BC8(ones),zero);
      z0=MFMA16(BC8(pk[1][0]),BC8(ones),z0); z1=MFMA16(BC8(pk[1][1]),BC8(ones),z1);
      f32x4 o0=MFMA16(BC8(pk[0][0]),BC8(vn0),zero), o1=MFMA16(BC8(pk[0][1]),BC8(vn0),zero);
      o0=MFMA16(BC8(pk[1][0]),BC8(vn1),o0); o1=MFMA16(BC8(pk[1][1]),BC8(vn1),o1);
      *(AS3 u32x4*)outbox=pk8(s0,s1);
      asm volatile("":::"memory");
      __hip_atomic_store(LF(wid^2),1u,RLX_WG);
      if(l15==0){
        unsigned zo=WS_ZP+((((c*2+wh)*2+wr)*16+r)*4+g4)*32;
        STB128(__builtin_bit_cast(u32x4,z0),zo,0);
        STB128(__builtin_bit_cast(u32x4,z1),zo+16,0);
      }
      u32x4 in;
      #pragma unroll 1
      for(unsigned it=0; it<(SPINCAP<<4); ++it){
        unsigned v=__hip_atomic_load(LF(wid),RLX_WG);
        asm volatile("":::"memory");
        in=*(AS3 u32x4*)inbox;
        asm volatile("":::"memory");
        if((unsigned)__builtin_amdgcn_readfirstlane(v)>=1u) break;
      }
      u32x4 v={pk2s(o0[0]+blo(in[0]),o0[1]+bhi(in[0])),pk2s(o0[2]+blo(in[1]),o0[3]+bhi(in[1])),pk2s(o1[0]+blo(in[2]),o1[1]+bhi(in[2])),pk2s(o1[2]+blo(in[3]),o1[3]+bhi(in[3]))};
      STB128(v,lane*16,WS_KVP+(((c*2+wh)*2+wr)*16+r)*1024);
    }
    SB();
    ST(3);
    SB();
    u32x4 pq[4];
    pq[0]=phi8(acc[0][0],acc[0][1]); pq[1]=phi8(acc[1][0],acc[1][1]);
    asm volatile("s_waitcnt vmcnt(0)":"+v"(pq[0]),"+v"(pq[1])::"memory");
    const bool last1=lds_arrive(LF(6),lane)==3;
    if(last1 && lane<16) __hip_atomic_store((gu32*)(ws+WS_FKV+bid_g1(lane,cq)*128+r*4),tag,RLX_AGENT);
    ST(4);
    pq[2]=phi8(acc[2][0],acc[2][1]); pq[3]=phi8(acc[3][0],acc[3][1]);
    PIN(pq[2]); PIN(pq[3]);
    u32x4 sel0={0,0,0,0}, sel1={0,0,0,0};
    { unsigned on=((l15>>2)==g4)?((l15&1)?0x3F800000u:0x00003F80u):0u;
      unsigned lo=(l15&2)?0u:on, hi=(l15&2)?on:0u;
      sel0[0]=lo; sel0[1]=hi; sel1[2]=lo; sel1[3]=hi; PIN(sel0); PIN(sel1); }
    if(last1){
      poll_all((gu32*)(ws+WS_FKV+bid*128+l15*4),tag);
      if(lane==0) __hip_atomic_store(LF(7),1u,RLX_WG);
    } else lds_wait_ge<1>(LF(7),1u);
    ST(5);
    u32x4 kvF[2], zF;
    {
      u32x4 P[16], Z[4];
      const unsigned ko=WS_KVP+((cq*2+wh)*2+wr)*16384;
      const unsigned zo=WS_ZP+(((cq*2+wh)*2)*16+l15)*128+g4*32;
      Z[0]=LDB128(zo,0); Z[1]=LDB128(zo+16,0); Z[2]=LDB128(zo+2048,0); Z[3]=LDB128(zo+2048+16,0);
      #pragma unroll
      for(int p=0;p<8;p++) P[p]=LDB128(lane*16+(p&3)*1024,ko+(((p>>2)+r)&3)*4096);
      SB();
      f32x4 d0={},d1={};
      #pragma unroll
      for(int p=0;p<8;p++){
        P[p+8]=LDB128(lane*16+(p&3)*1024,ko+(((p>>2)+2+r)&3)*4096);
        d0=MFMA16(BC8(sel0),BC8(P[p]),d0); d1=MFMA16(BC8(sel1),BC8(P[p]),d1);
        SG(0x8,2); SG(0x20,1);
      }
      SB();
      float z[8];
      #pragma unroll
      for(int i=0;i<8;i++){ float t=u2f(Z[i>>2][i&3])+u2f(Z[2+(i>>2)][i&3]); asm("":"+v"(t)); z[i]=row_sum16(t); }
      zF=u32x4{pk2s(z[0],z[1]),pk2s(z[2],z[3]),pk2s(z[4],z[5]),pk2s(z[6],z[7])};
      PIN(zF);
      SB();
      #pragma unroll
      for(int p=8;p<16;p++){ d0=MFMA16(BC8(sel0),BC8(P[p]),d0); d1=MFMA16(BC8(sel1),BC8(P[p]),d1); }
      u32x4 own=pk8(d0,d1);
      *(AS3 u32x4*)outbox=own;
      asm volatile("":::"memory");
      __hip_atomic_store(LF(wid^2),2u,RLX_WG);
      u32x4 oth;
      #pragma unroll 1
      for(unsigned it=0; it<(SPINCAP<<4); ++it){
        unsigned v=__hip_atomic_load(LF(wid),RLX_WG);
        asm volatile("":::"memory");
        oth=*(AS3 u32x4*)inbox;
        asm volatile("":::"memory");
        if((unsigned)__builtin_amdgcn_readfirstlane(v)>=2u) break;
      }
      kvF[0]=wr?oth:own; kvF[1]=wr?own:oth;
    }
    ST(6);
    {
      const f32x4 zero={};
      f32x4 den[4],n0[4],n1[4];
      #pragma unroll
      for(int m=0;m<4;m++) den[m]=MFMA16(BC8(zF),BC8(pq[m]),zero);
      #pragma unroll
      for(int m=0;m<4;m++){ n0[m]=MFMA16(BC8(kvF[0]),BC8(pq[m]),zero); n1[m]=MFMA16(BC8(kvF[1]),BC8(pq[m]),zero); }
      const unsigned vo=(g4>>1)*1024+((g4&1)*32+l15)*16;
      #pragma unroll
      for(int m=0;m<4;m++){
        float rc=__builtin_amdgcn_rcpf(den[m][0]+EPS);
        u32x4 v={pk2s(n0[m][0]*rc,n0[m][1]*rc),pk2s(n0[m][2]*rc,n0[m][3]*rc),pk2s(n1[m][0]*rc,n1[m][1]*rc),pk2s(n1[m][2]*rc,n1[m][3]*rc)};
        STB128(v,vo+(m&1)*256,WS_OF+((4*r+2*wr+(m>>1))*32+2*cq+wh)*2048);
      }
    }
    ST(7);
    lds_wait_ge<1>(LF(4),4u);
    WAITV(0);
    const bool last2 = lds_arrive(LF(8),lane)==3;
    if(last2 && lane<16) __hip_atomic_store((gu32*)(ws+WS_FO+bid_g2(r,lane)*128+c*4),tag,RLX_AGENT);
    ST(8);
    const int hh=lane>>5, l31=lane&31;
    unsigned ao[2];
    { int q=l15>>2,p=lane&3,blk=g4&1;
      #pragma unroll
      for(int n=0;n<2;n++) ao[n]=(4*hh+q)*128+(((4*n+2*blk+(p>>1))^(((q>>1)&1)<<2))*16)+8*(p&1); }
    AS3 char* aE[2]={smem+L_E+ao[0],smem+L_E+ao[1]};
    AS3 char* a1[2]={smem+SLOT+ao[0],smem+SLOT+ao[1]};
    AS3 char* a0[2]={smem+ao[0],smem+ao[1]};
    AS3 char* a2_[2]={smem+2*SLOT+R_WO+ao[0],smem+2*SLOT+R_WO+ao[1]};
    PIN(aE[0]);PIN(aE[1]);PIN(a1[0]);PIN(a1[1]);PIN(a0[0]);PIN(a0[1]);PIN(a2_[0]);PIN(a2_[1]);
    bf16x8 A[2][2][2];
    auto rdA=[&](int h) AI {
      #pragma unroll
      for(int ks=0;ks<2;ks++)
      #pragma unroll
      for(int n=0;n<2;n++){
        AS3 char* bw = h<9 ? aE[n]+4096*h+1024*ks : h<19 ? a1[n]+4096*(h-9)+1024*ks : h<29 ? a0[n]+4096*(h-19)+1024*ks : a2_[n]+1024*(h-29)+10240*ks;
        A[h&1][ks][n]=CAT8(TR16(bw),TR16(bw+(h>=29?20480:2048))); }
    };
    rdA(0);
    if(last2){
      poll_all((gu32*)(ws+WS_FO+bid*128+l15*4),tag);
      if(lane==0) __hip_atomic_store(LF(9),1u,RLX_WG);
      if(bid==0 && lane==0) __hip_atomic_store((gu32*)(ws+WS_EPOCH),tag,RLX_AGENT);
    } else lds_wait_ge<1>(LF(9),1u);
    ST(9);
    f32x16 a2[2]={};
    const unsigned oo=WS_OF+(4*r2+wid)*65536;
    const int t0=128*r2+32*wid;
#define PF 6
    u32x4 B[32][2];
    auto ldB=[&](int h) AI {
      const unsigned so=oo+(((h>>1)+hp)&15)*4096;
      B[h][0]=LDB128(lane*16+(h&1)*2048,so);
      B[h][1]=LDB128(lane*16+(h&1)*2048+1024,so);
    };
    #pragma unroll
    for(int h=0;h<PF;h++) ldB(h);
    __builtin_amdgcn_s_setprio(0);
    SB();
    u32x4 bt[2][2]; float gv; unsigned fb;
    #pragma unroll
    for(int h=0;h<32;h++){
      if(h==18){ lds_wait_ge<0>(LF(11),4u); SB(); }
      if(h==32-PF-6){ fb=__hip_atomic_load((gu32*)(ws+WS_FB+bid*128+l15*4),RLX_AGENT); SB(); }
      if(h==32-PF){
        if(__builtin_amdgcn_ballot_w64(fb!=tag)!=0ull) poll_all((gu32*)(ws+WS_FB+bid*128+l15*4),tag);
        asm volatile("":::"memory");
        #pragma unroll
        for(int n=0;n<2;n++)
        #pragma unroll
        for(int ks=0;ks<2;ks++) bt[n][ks]=LDB128(l31*4096+hh*16+ks*32,WS_BT+(64*c2+32*n)*4096+t0*2);
        gv=u2f(__hip_atomic_load((gu32*)(ws+WS_G+(t0+l31)*4),RLX_AGENT));
        SB();
      }
      if(h==1){ST(12);} if(h==8){ST(13);} if(h==16){ST(14);} if(h==24){ST(15);}
      if(h+1<32) rdA(h+1);
      if(h+PF<32) ldB(h+PF);
      #pragma unroll
      for(int ks=0;ks<2;ks++)
      #pragma unroll
      for(int n=0;n<2;n++) a2[n]=MFMA32(A[h&1][ks][n],BC8(B[h][ks]),a2[n]);
      SG(0x8,1);SG(0x100,3);SG(0x8,1);SG(0x100,3);SG(0x8,1);SG(0x100,2);SG(0x20,1);SG(0x8,1);SG(0x20,1);
      SB();
    }
    ST(10);
    {
      u32x4 sel[2];
      #pragma unroll
      for(int ks=0;ks<2;ks++){
        unsigned on=((l31>>3)==2*ks+hh)?((l31&1)?0x3F800000u:0x00003F80u):0u; int dw=(l31&7)>>1;
        sel[ks]=u32x4{dw==0?on:0u,dw==1?on:0u,dw==2?on:0u,dw==3?on:0u}; }
      char* op=out+(size_t)(t0+l31)*2048+c2*128+hh*16;
      const float g=gv, h1=rbf(1.f-g);
      #pragma unroll
      for(int n=0;n<2;n++){
        const f32x16 zero={};
        f32x16 bb=MFMA32(BC8(bt[n][0]),BC8(sel[0]),zero); bb=MFMA32(BC8(bt[n][1]),BC8(sel[1]),bb);
        f32x16 o=a2[n]*g+bb*h1;
        #pragma unroll
        for(int k=0;k<4;k+=2){
          unsigned ax_=pk2(o[4*k],o[4*k+1]), ay=pk2(o[4*k+2],o[4*k+3]), bx=pk2(o[4*k+4],o[4*k+5]), by=pk2(o[4*k+6],o[4*k+7]);
          auto r0=__builtin_amdgcn_permlane32_swap(ax_,bx,false,false); auto r1=__builtin_amdgcn_permlane32_swap(ay,by,false,false);
          u32x4 v={r0[0],r1[0],r0[1],r1[1]};
          *(u32x4*)(op+n*64+k*16)=v;
        }
      }
    }
  }
}
extern "C" void kernel_launch(void* const* d_in, const int* in_sizes, int n_in,
                              void* d_out, int out_size, void* d_ws, size_t ws_size,
                              hipStream_t stream) {
  (void)hipFuncSetAttribute((const void*)fused,hipFuncAttributeMaxDynamicSharedMemorySize,LDS_BYTES);
  fused<<<256,512,LDS_BYTES,stream>>>((const char*)d_in[0],(const char*)d_in[1],(const char*)d_in[2],
     (const char*)d_in[4],(const char*)d_in[5],(const char*)d_in[6],(const char*)d_in[7],
     (const char*)d_in[8],(const char*)d_in[9],(const char*)d_in[10],(char*)d_out,(char*)d_ws);
}
```

```cpp
#include <hip/hip_runtime.h>
#include <hip/hip_bf16.h>
#include <type_traits>
#include <utility>
#define AS1 __attribute__((address_space(1)))
#define AS3 __attribute__((address_space(3)))
#define DI __device__ __forceinline__
#define AI __attribute__((always_inline))
typedef __bf16 bf16x2 __attribute__((ext_vector_type(2)));
typedef __bf16 bf16x4 __attribute__((ext_vector_type(4)));
typedef __bf16 bf16x8 __attribute__((ext_vector_type(8)));
typedef float f32x2 __attribute__((ext_vector_type(2)));
typedef float f32x4 __attribute__((ext_vector_type(4)));
typedef float f32x16 __attribute__((ext_vector_type(16)));
typedef unsigned u32x2 __attribute__((ext_vector_type(2)));
typedef unsigned u32x4 __attribute__((ext_vector_type(4)));
typedef AS1 unsigned gu32;

constexpr int SLOT=40960, NST=16, LDS_BYTES=163840;
constexpr unsigned TAGINC=0x9E3779B9u;
constexpr unsigned SPINCAP=1u<<18;
constexpr unsigned WS_EPOCH=0;
constexpr unsigned WS_G  =4096;
constexpr unsigned WS_FKV=65536;
constexpr unsigned WS_FO =WS_FKV+32768;
constexpr unsigned WS_FX =WS_FO+32768;
constexpr unsigned WS_FB =WS_FX+32768;
constexpr unsigned WS_ZP =262144;
constexpr unsigned WS_KVP=1u<<20;
constexpr unsigned WS_OF =2u<<20;
constexpr unsigned WS_XT =6u<<20;
constexpr unsigned WS_BT =10u<<20;
constexpr unsigned WS_END=14u<<20;
constexpr unsigned WS_DBG=16u<<20;
constexpr unsigned L_E=122880, L_GW=159744, L_FLAG=161792;
constexpr unsigned R_KC=4576, R_WO=5888, R_IN=9216, Q_P1=4160;
#define SPS 5
#define LF(i) ((AS3 unsigned*)(smem+L_FLAG+(i)*4))

DI void glds16(const void* gbase, unsigned voff, unsigned lds){
  asm volatile("s_mov_b32 m0, %2\n\ts_nop 0\n\tglobal_load_lds_dwordx4 %0, %1"
     :: "v"(voff), "s"(gbase), "s"(lds) : "memory");
}
#define WAITV(n) asm volatile("s_waitcnt vmcnt(" #n ")":::"memory")
#define WAITL() asm volatile("s_waitcnt lgkmcnt(0)":::"memory")
#define BAR_L() asm volatile("s_waitcnt lgkmcnt(0)\n\ts_barrier":::"memory")
#define MFMA16(a,b,c) __builtin_amdgcn_mfma_f32_16x16x32_bf16((a),(b),(c),0,0,0)
#define MFMA32(a,b,c) __builtin_amdgcn_mfma_f32_32x32x16_bf16((a),(b),(c),0,0,0)
#define SB() __builtin_amdgcn_sched_barrier(0)
#define SG(m,n) __builtin_amdgcn_sched_group_barrier(m,n,0)
#define RLX_AGENT __ATOMIC_RELAXED, __HIP_MEMORY_SCOPE_AGENT
#define RLX_WG __ATOMIC_RELAXED, __HIP_MEMORY_SCOPE_WORKGROUP
#define BC8(x) __builtin_bit_cast(bf16x8,(x))
#define TR16(p) __builtin_amdgcn_ds_read_tr16_b64_v4bf16((AS3 bf16x4*)(p))
#define CAT8(a,b) __builtin_shufflevector((a),(b),0,1,2,3,4,5,6,7)
#define LDB128(off,soff) __builtin_amdgcn_raw_buffer_load_b128(rs,(off),(soff),16)
#define STB128(v,off,soff) __builtin_amdgcn_raw_buffer_store_b128((v),rs,(off),(soff),16)
#define PIN(x) asm volatile("":"+v"(x))

#define ST(i)
constexpr float L2E=1.44269504088896f;
constexpr float EPS=1e-6f, ACC0=1.f+1e-6f;

DI unsigned pk2(float a,float b){ f32x2 t={a,b}; bf16x2 p=__builtin_convertvector(t,bf16x2); return __builtin_bit_cast(unsigned,p);}
DI unsigned pk2s(float a,float b){ asm("":"+v"(a)); asm("":"+v"(b)); return pk2(a,b); }
DI u32x4 pk8(const f32x4&a,const f32x4&b){ u32x4 r; r[0]=pk2(a[0],a[1]); r[1]=pk2(a[2],a[3]); r[2]=pk2(b[0],b[1]); r[3]=pk2(b[2],b[3]); return r; }
DI float u2f(unsigned u){ return __builtin_bit_cast(float,u); }
DI float blo(unsigned w){ return u2f(w<<16); }
DI float bhi(unsigned w){ return u2f(w&0xffff0000u); }
DI float rbf(float v){ return blo(pk2(v,v)); }
DI float phi1(float a){
  float e=__builtin_amdgcn_fmed3f(__builtin_amdgcn_exp2f(__builtin_fmaf(a,L2E,-L2E*ACC0)),0.f,1.f)+EPS;
  asm("":"+v"(e));
  int x=__builtin_bit_cast(int,a), y=__builtin_bit_cast(int,e); return __builtin_bit_cast(float,x>y?x:y); }
DI u32x4 phi8(const f32x4&a,const f32x4&b){ u32x4 r; r[0]=pk2(phi1(a[0]),phi1(a[1])); r[1]=pk2(phi1(a[2]),phi1(a[3])); r[2]=pk2(phi1(b[0]),phi1(b[1])); r[3]=pk2(phi1(b[2]),phi1(b[3])); return r; }
DI void poll_all(const gu32* p, unsigned tag){
  #pragma unroll 1
  for(unsigned it=0; it<SPINCAP; ++it){
    unsigned v=__hip_atomic_load(p, RLX_AGENT);
    if(__builtin_amdgcn_ballot_w64(v!=tag)==0ull) break;
    __builtin_amdgcn_s_sleep(1);
  }
  asm volatile("":::"memory");
}
template<int SL> DI void lds_wait_ge(AS3 unsigned* p, unsigned want){
  #pragma unroll 1
  for(unsigned it=0; it<(SPINCAP<<4); ++it){
    unsigned v=__hip_atomic_load(p,RLX_WG);
    if((unsigned)__builtin_amdgcn_readfirstlane(v)>=want) break;
    if(SL) __builtin_amdgcn_s_sleep(SL);
  }
  asm volatile("":::"memory");
}
DI unsigned lds_arrive(AS3 unsigned* p,int lane){
  unsigned old=0; if(lane==0) old=__hip_atomic_fetch_add(p,1u,RLX_WG);
  return __builtin_amdgcn_readfirstlane(old);
}
template<int CTRL> DI float dpp_add(float x){
  int y=__builtin_amdgcn_update_dpp(0,__builtin_bit_cast(int,x),CTRL,0xf,0xf,true);
  return x+__builtin_bit_cast(float,y);
}
DI float row_sum16(float x){ return dpp_add<0x121>(dpp_add<0x122>(dpp_add<0x124>(dpp_add<0x128>(x)))); }
template<int N> using IC=std::integral_constant<int,N>;
template<class F,int...I> DI void sfor_(F&&f,std::integer_sequence<int,I...>){ (f(IC<I>{}),...); }
template<int N,class F> DI void sfor(F&&f){ sfor_(f,std::make_integer_sequence<int,N>{}); }
struct Cx{ float r0,i0,r1,i1; };
#define PINC(v) asm volatile("":"+v"(v.r0),"+v"(v.i0),"+v"(v.r1),"+v"(v.i1))
DI Cx cmul(const Cx&a,const Cx&b){ Cx r;
  r.r0=__builtin_fmaf(a.r0,b.r0,-(a.i0*b.i0)); r.i0=__builtin_fmaf(a.r0,b.i0,a.i0*b.r0);
  r.r1=__builtin_fmaf(a.r1,b.r1,-(a.i1*b.i1)); r.i1=__builtin_fmaf(a.r1,b.i1,a.i1*b.r1);
  PINC(r); return r; }
DI void cexp1(float lr,float rev,float p,float&re,float&im){
  float e=__builtin_amdgcn_exp2f(lr*p); float f=__builtin_amdgcn_fractf(rev*p);
  re=e*__builtin_amdgcn_cosf(f); im=e*__builtin_amdgcn_sinf(f); asm volatile("":"+v"(re),"+v"(im));
}
DI int bid_g1(int r,int c){ return (((r&7)|((c&3)<<3))<<3)|(r>>3)|((c>>2)<<1); }
#define MAP2 1
DI int bid_g2(int r2,int c2){ return ((((r2&3)<<3)|(c2&7))<<3)|((r2>>2)<<1)|(c2>>3); }

__global__ __launch_bounds__(512,2) void fused(const char* __restrict__ x, const char* __restrict__ w, const char* __restrict__ wo,
     const char* __restrict__ pla, const char* __restrict__ pai, const char* __restrict__ pcr, const char* __restrict__ pci,
     const char* __restrict__ pdt, const char* __restrict__ pdk, const char* __restrict__ pgw,
     char* __restrict__ out, char* ws){
  extern __shared__ __attribute__((aligned(16))) char smem_[];
  AS3 char* smem=(AS3 char*)smem_;
  const int tid=threadIdx.x, lane=tid&63;
  const int wid=__builtin_amdgcn_readfirstlane(tid>>6);
  const int bid=blockIdx.x, g8=bid&7, idx=bid>>3;
  const int r=(g8&1)*8+(idx&7), c=(g8>>1)*4+(idx>>3);
  const int r2=4*(g8>>1)+(idx>>3), c2=8*(g8&1)+(idx&7), hp=(2*(idx&7)+4*(idx>>3))&15;
  const int g4=lane>>4, l15=lane&15;
#define QX 1
  const int cq=c^QX;
  ST(0);
  asm volatile(""::"s"(x),"s"(w),"s"(wo),"s"(pla),"s"(pai),"s"(pcr),"s"(pci),"s"(pdt),"s"(pdk),"s"(pgw),"s"(out),"s"(ws));
  const unsigned lbase=(unsigned)(size_t)smem;
  const __amdgpu_buffer_rsrc_t rs=__builtin_amdgcn_make_buffer_rsrc(ws,0,WS_END,0x00020000);
  const unsigned vwo=(lane>>3)*2048+(((lane&7)^(((lane>>4)&1)<<2))*16);
  const char* wob=wo+c2*128;
  #define GO(p,dst) glds16(wob+(size_t)(((p)+8*hp)&127)*16384,vwo,lbase+(dst))
  if(__builtin_expect(wid>=4,1)){
    const int lw=wid-4;
    const int ch=64*c+4*r+lw;
    AS3 char* R=smem+2*SLOT+lw*10240;
    AS3 char* Q=smem+lw*10240;
    const int xb8=(4*(g8>>1)+8*(g8&1))&15, rho=(r+c)&1, rho4=(r+c)&3;
    unsigned vx[4], vw[6];
    #pragma unroll
    for(int i=0;i<4;i++){ int row=8*(4*lw+i)+(lane>>3); int cc=(lane&7)^((row>>1)&7); vx[i]=row*2048+cc*16; }
    #pragma unroll
    for(int i=0;i<6;i++){ int p=6*lw+i; int seg=p>>3, kr=(p&7)*8+(lane>>3); int cc=(lane&7)^((((kr>>1)&1)|(((kr>>3)&1)<<1))<<1); vw[i]=kr*6144+seg*2048+cc*16+(seg==0?cq:c)*128; }
    const char* xb=x+(size_t)r*128*2048;
    const char* wb=w;
    #define GX(i) glds16(xs,vx[i],ls+(4*lw+i)*1024)
    #define GW(i) glds16(wsb,vw[i],ls+16384+(6*lw+i)*1024)
#define MIX 16
    #define KC(s) ((xb8+((s)<MIX ? ((s)^rho) : ((s)^rho4)))&15)
    #define ISSUE_SETUP(s) const unsigned ls=lbase+((s)%3)*SLOT; const int kc=KC(s); const char* xs=xb+kc*128; const char* wsb=wb+(size_t)kc*64*6144;
    auto issue=[&](int s,int) AI { ISSUE_SETUP(s) GW(0);GX(0);GW(1);GW(2);GX(1);GW(3);GX(2);GW(4);GW(5);GX(3); };
    issue(0,0);
    ST(6);
    unsigned q_la,q_ai,q_cr,q_ci,q_dt,q_dk,q_ep;
    { unsigned o=ch*256+lane*4, o2=ch*2, z=0;
      asm volatile("global_load_dword %0, %1, %2":"=v"(q_la):"v"(o),"s"(pla):"memory");
      asm volatile("global_load_dword %0, %1, %2":"=v"(q_ai):"v"(o),"s"(pai):"memory");
      asm volatile("global_load_dword %0, %1, %2":"=v"(q_cr):"v"(o),"s"(pcr):"memory");
      asm volatile("global_load_dword %0, %1, %2":"=v"(q_ci):"v"(o),"s"(pci):"memory");
      asm volatile("global_load_ushort %0, %1, %2":"=v"(q_dt):"v"(o2),"s"(pdt):"memory");
      asm volatile("global_load_ushort %0, %1, %2":"=v"(q_dk):"v"(o2),"s"(pdk):"memory");
      asm volatile("global_load_dword %0, %1, %2 sc1":"=v"(q_ep):"v"(z),"s"(ws):"memory");
      if(lw==3){ glds16(pgw,lane*16,lbase+L_GW); glds16(pgw+1024,lane*16,lbase+L_GW+1024); } }
    const int xchunk=xb8+(c&3), xstage=(c&3)^rho;
    bf16x8 XT[4]; float gs=0.f;
    const unsigned gxo=(8*c+2*lw+(lane>>5))*128+((((lane&31)>>2)^((4*c+lw)&7))*16)+(lane&3)*4;
    auto gate=[&](int s,int slot) AI {
      const int kc=KC(s);
      unsigned xa=*(AS3 unsigned*)(smem+slot*SLOT+gxo), ga=*(AS3 unsigned*)(smem+L_GW+kc*128+(lane&31)*4);
      gs=__builtin_fmaf(blo(xa),blo(ga),gs); gs=__builtin_fmaf(bhi(xa),bhi(ga),gs); PIN(gs);
    };
    issue(1,1);
    asm volatile("s_waitcnt vmcnt(10)\n\ts_barrier":"+v"(q_la),"+v"(q_ai),"+v"(q_cr),"+v"(q_ci),"+v"(q_dt),"+v"(q_dk),"+v"(q_ep)::"memory");
    const unsigned tag=__builtin_amdgcn_readfirstlane(q_ep)+TAGINC;
    u32x2 TB[57]; Cx w1,w2,w4,w8c,w128,G,E; float lr0,lr1,rv0,rv1;
    auto put=[&](int e,const Cx&v) AI { TB[e]=u32x2{pk2(v.r0,v.i0),pk2(v.r1,v.i1)}; PIN(TB[e]); };
    auto step=[&](auto KK) AI { constexpr int k=decltype(KK)::value;
      if constexpr(k==0){ const float dt=rbf(__builtin_amdgcn_exp2f(blo(q_dt)*L2E));
        lr0=-rbf(__builtin_amdgcn_exp2f(blo(q_la)*L2E))*(dt*L2E); PIN(lr0);
        lr1=-rbf(__builtin_amdgcn_exp2f(bhi(q_la)*L2E))*(dt*L2E); PIN(lr1);
        rv0=dt; PIN(rv0); }
      else if constexpr(k==1){ const float dt=rv0;
        rv0=blo(q_ai)*(dt*0.159154943091895f); PIN(rv0); rv1=bhi(q_ai)*(dt*0.159154943091895f); PIN(rv1);
        G.r0=blo(q_cr)*(2.f*dt); G.i0=blo(q_ci)*(2.f*dt); G.r1=bhi(q_cr)*(2.f*dt); G.i1=bhi(q_ci)*(2.f*dt); PINC(G); }
      else if constexpr(k==2){ cexp1(lr0,-rv0,8.f,w8c.r0,w8c.i0); }
      else if constexpr(k==3){ cexp1(lr1,-rv1,8.f,w8c.r1,w8c.i1); }
      else if constexpr(k==4){ cexp1(lr0,rv0,1.f,w1.r0,w1.i0); }
      else if constexpr(k==5){ cexp1(lr1,rv1,1.f,w1.r1,w1.i1); }
      else if constexpr(k==6){ cexp1(lr0,rv0,128.f,w128.r0,w128.i0); }
      else if constexpr(k==7){ cexp1(lr1,rv1,128.f,w128.r1,w128.i1); }
      else if constexpr(k==8){ w2=cmul(w1,w1); }
      else if constexpr(k==9){ w4=cmul(w2,w2); }
      else if constexpr(k==10){ TB[0]=u32x2{0x3F80u,0x3F80u}; E=w8c; put(1,E); }
      else if constexpr(k>=11 && k<25){ E=cmul(E,w8c); put(k-9,E); }
      else if constexpr(k>=25 && k<41){ constexpr int e=k-25; if constexpr((e&7)==0){ E=G; G=cmul(G,w128); } else E=cmul(E,w1); put(16+e,E); }
      else if constexpr(k>=41 && k<49){ constexpr int e=k-41; if constexpr((e&3)==0){ E=G; G=cmul(G,w128); } else E=cmul(E,w2); put(32+e,E); }
      else if constexpr(k>=49 && k<57){ constexpr int e=k-41; if constexpr((e&1)==0){ E=G; G=cmul(G,w128); } else E=cmul(E,w4); put(32+e,E); }
      else if constexpr(k>=57 && k<66){ put(48+k-57,G); if constexpr(k<65) G=cmul(G,w128); }
    };
#define S0 1
    #define STEP(s,j) do{ if constexpr((j)<SPS && (s)>=S0) step(IC<SPS*((s)-S0)+(j)>{}); }while(0)
    sfor<NST-2>([&](auto SS) AI { constexpr int s=decltype(SS)::value;
      { ISSUE_SETUP(s+2)
        GW(0);GX(0);GW(1);GW(2);GX(1);GW(3);STEP(s,0);GX(2);GW(4);STEP(s,1);GW(5);GX(3);STEP(s,2);STEP(s,3);
      }
      STEP(s,4);STEP(s,5);
      if(s<4 && s==xstage){
        int q=l15>>2,p=lane&3;
        AS3 char* b=smem+(s%3)*SLOT+(8*g4+q)*128+8*(p&1);
        AS3 char* b0=b+(((2*lw+(p>>1))^((4*g4+(q>>1))&7))*16);
        AS3 char* b1=b+512+(((2*lw+(p>>1))^((4*g4+2+(q>>1))&7))*16);
        #pragma unroll
        for(int j=0;j<4;j++) XT[j]=CAT8(TR16(b0+j*4096),TR16(b1+j*4096));
      }
      gate(s,s%3);
      asm volatile("s_waitcnt vmcnt(10) lgkmcnt(0)\n\ts_barrier":::"memory");
    });
    ST(1);
    u32x4 WC[10];
    #define GC(i) asm volatile("global_load_dwordx4 %0, %1, %2":"=v"(WC[i]):"v"(vwo),"s"(wob+(size_t)((76+10*lw+(i)+8*hp)&127)*16384):"memory")
    {
      AS3 char* kgw=R+lane*8;
      AS3 char* kgr=R+l15*528+g4*16;
      bf16x8 OF[8],TF[8],T2[8]; f32x4 kt0={},kt1={},kt2={};
      { unsigned o=WS_XT+((4*xchunk+lw)*16+r)*4096+l15*64+g4*16;
        #pragma unroll
        for(int j=0;j<4;j++) STB128(__builtin_bit_cast(u32x4,XT[j]),o+j*1024,0); }
      GC(0);GC(1);GC(2);GC(3);GC(4);
      STEP(14,0);STEP(14,1);STEP(14,2);STEP(14,3);STEP(14,4);STEP(14,5);
      gate(14,2);
      asm volatile("s_waitcnt vmcnt(9) lgkmcnt(0)\n\ts_barrier":::"memory");
      ST(2);
      GC(5);GC(6);GC(7);GC(8);GC(9);
      gate(15,0);
      if(lane==0) __hip_atomic_fetch_add(LF(13),1u,RLX_WG);
      sfor<66>([&](auto KK) AI { constexpr int k=decltype(KK)::value; if constexpr(k>=SPS*(15-S0)) step(KK); });
      #define W4(b,e) do{ *(AS3 u32x2*)(kgw+(e)*528)=TB[(b)+(e)]; *(AS3 u32x2*)(kgw+((e)+1)*528)=TB[(b)+(e)+1]; *(AS3 u32x2*)(kgw+((e)+2)*528)=TB[(b)+(e)+2]; *(AS3 u32x2*)(kgw+((e)+3)*528)=TB[(b)+(e)+3]; }while(0)
      W4(0,0); W4(0,4);
      WAITV(10);
      if(lane<4) __hip_atomic_store((gu32*)(ws+WS_FX+bid_g1(4*lw+lane,xchunk)*128+r*4),tag,RLX_AGENT);
      W4(0,8); W4(0,12);
      #pragma unroll
      for(int t=0;t<8;t++) OF[t]=*(AS3 bf16x8*)(kgr+t*64);
      W4(16,0); W4(16,4); W4(16,8); W4(16,12);
      #pragma unroll
      for(int t=0;t<8;t++) TF[t]=*(AS3 bf16x8*)(kgr+t*64);
      {
        gs=row_sum16(gs); gs+=u2f(__builtin_amdgcn_permlane16_swap(__builtin_bit_cast(unsigned,gs),__builtin_bit_cast(unsigned,gs),false,false)[1]);
        float g=rbf(__builtin_amdgcn_rcpf(1.f+__builtin_amdgcn_exp2f(-rbf(gs)*L2E)));
        if((lane&31)==0) __hip_atomic_store((gu32*)(ws+WS_G+(128*r+8*c+2*lw+(lane>>5))*4),__builtin_bit_cast(unsigned,g),RLX_AGENT);
      }
      W4(32,0); W4(32,4); W4(32,8);
      unsigned fx; { unsigned fo=WS_FX+bid*128+l15*4; asm volatile("global_load_dword %0, %1, %2 sc1":"=v"(fx):"v"(fo),"s"(ws):"memory"); }
      W4(32,12);
      #pragma unroll
      for(int t=0;t<8;t++) kt0=MFMA16(TF[t],OF[t],kt0);
      #pragma unroll
      for(int t=0;t<8;t++) TF[t]=*(AS3 bf16x8*)(kgr+t*64);
      W4(48,0); W4(48,4); *(AS3 u32x2*)(kgw+8*528)=TB[56];
      #pragma unroll
      for(int t=0;t<8;t++) T2[t]=*(AS3 bf16x8*)(kgr+t*64);
      ST(8);
      asm volatile("s_waitcnt vmcnt(0) lgkmcnt(0)":"+v"(fx)::"memory");
      asm volatile("":"+v"(WC[0]),"+v"(WC[1]),"+v"(WC[2]),"+v"(WC[3]),"+v"(WC[4]),"+v"(WC[5]),"+v"(WC[6]),"+v"(WC[7]),"+v"(WC[8]),"+v"(WC[9]));
      if(__builtin_amdgcn_ballot_w64(fx!=tag)!=0ull) poll_all((gu32*)(ws+WS_FX+bid*128+l15*4),tag);
      { const char* ub=ws+WS_XT+(size_t)(ch>>4)*65536+(ch&15)*64; const unsigned o=(lane>>2)*1024+(lane&3)*16;
        #pragma unroll
        for(int i=0;i<4;i++) asm volatile("s_mov_b32 m0, %2\n\ts_nop 0\n\tglobal_load_lds_dwordx4 %0, %1 sc1"::"v"(o),"s"(ub+i*16384),"s"(lbase+2*SLOT+lw*10240+480+i*1024):"memory");
        #pragma unroll
        for(int i=0;i<3;i++) GO(116+4*i+lw,2*SLOT+lw*10240+R_WO+i*1024); }
      #pragma unroll
      for(int t=0;t<8;t++){ kt1=MFMA16(TF[t],OF[t],kt1); kt2=MFMA16(OF[t],T2[t],kt2); }
      AS3 char* kc=R+R_KC;
      u32x2 t0={pk2(kt0[0],kt0[1]),pk2(kt0[2],kt0[3])};
      *(AS3 u32x2*)(kc+2*(128*(g4>>1)+8*l15+4*(g4&1)))=t0;
      int a0= g4<2 ? 256+64*g4+4*l15 : 384+64*(g4-2)+2*l15;
      int a1= g4<2 ? a0+2 : a0+32;
      *(AS3 unsigned*)(kc+2*a0)=pk2(kt1[0],kt1[1]);
      *(AS3 unsigned*)(kc+2*a1)=pk2(kt1[2],kt1[3]);
      if(l15<9){ u32x2 t2={pk2(kt2[0],kt2[1]),pk2(kt2[2],kt2[3])}; *(AS3 u32x2*)(kc+2*(512+16*l15+4*g4))=t2; }
    }
    ST(7);
    lds_wait_ge<0>(LF(13),8u);
    {
      AS3 char* kc=R+R_KC;
      const int be=63-lane;
      const int b1=be|32, b0=be&31;
      u32x2 e0=*(AS3 u32x2*)(kc+2*(128+4*b1)); unsigned c4=*(AS3 unsigned short*)(kc+2*(132+4*b1));
      u32x4 f0=*(AS3 u32x4*)(kc+16*b0); unsigned pm=*(AS3 unsigned short*)(kc+16*(lane<31?32:be)-2);
      unsigned e1=*(AS3 unsigned*)(kc+2*(384+2*be)); unsigned c2_=*(AS3 unsigned short*)(kc+2*(386+2*be));
      unsigned c20=*(AS3 unsigned short*)(kc+2*(512+be)), c21=*(AS3 unsigned short*)(kc+2*(513+be));
      unsigned c30=*(AS3 unsigned short*)(kc+2*(576+be)), c31=*(AS3 unsigned short*)(kc+2*(577+be));
      { u32x4 z={0,0,0,0};
        if(lane<30) *(AS3 u32x4*)(R+lane*16)=z;
        if(lane<2){ *(AS3 u32x4*)(Q+4096+lane*16)=z; *(AS3 u32x4*)(Q+Q_P1+4096+lane*16)=z; } }
      #pragma unroll
      for(int chi=0;chi<4;chi++){
        u32x4 d; unsigned r8;
        if(chi==0){
          const bool hi=lane<32;
          d[0]=hi?((e0[1]&0xffff0000u)|c4):__builtin_amdgcn_alignbit(f0[3],f0[3],16);
          d[1]=hi?__builtin_amdgcn_perm(e0[1],e0[1],0x01000100u):__builtin_amdgcn_alignbit(f0[2],f0[2],16);
          d[2]=hi?((e0[0]&0xffff0000u)|(e0[1]&0xffffu)):__builtin_amdgcn_alignbit(f0[1],f0[1],16);
          d[3]=hi?__builtin_amdgcn_perm(e0[0],e0[0],0x01000100u):__builtin_amdgcn_alignbit(f0[0],f0[0],16);
          r8= lane<31 ? e0[0] : lane==63 ? 0u : pm;
        } else if(chi==1){
          d[0]=c2_|(c2_<<16); d[1]=__builtin_amdgcn_perm(e1,e1,0x03020302u); d[2]=__builtin_amdgcn_alignbit(e1,e1,16); d[3]=__builtin_amdgcn_perm(e1,e1,0x01000100u);
          r8=e1;
        } else {
          unsigned c0=chi==2?c20:c30, c1=chi==2?c21:c31;
          d[0]=c1|(c1<<16); d[1]=c1|(((be&1)?c1:c0)<<16); d[2]=c0|(c0<<16); d[3]=d[2];
          r8=c0;
        }
        u32x4 f;
        f[0]=__builtin_amdgcn_alignbit(d[1],d[0],16); f[1]=__builtin_amdgcn_alignbit(d[2],d[1],16);
        f[2]=__builtin_amdgcn_alignbit(d[3],d[2],16); f[3]=__builtin_amdgcn_alignbit(r8,d[3],16);
        *(AS3 u32x4*)(Q+(3-chi)*1024+lane*16)=d; *(AS3 u32x4*)(Q+Q_P1+(3-chi)*1024+lane*16)=f;
      }
    }
    ST(9);
    WAITV(0);
    if(lane==0) __hip_atomic_fetch_add(LF(4),1u,RLX_WG);
    __builtin_amdgcn_s_setprio(1);
    f32x4 y[8]={};
    {
      const int n0=15-l15+8*g4;
      AS3 char* ak=Q+((n0&1)?Q_P1:0)+4*(n0>>1)+448;
      AS3 char* bu=R+32*l15+16*g4+448;
      u32x4 A0[8],A1[8]; bf16x8 B0[8],B1[8];
      auto ld=[&](u32x4* A,bf16x8* B,int off) AI {
        #pragma unroll
        for(int k=0;k<8;k++){ AS3 unsigned* p=(AS3 unsigned*)(ak+512*(7-k)-off); A[k]=u32x4{p[0],p[1],p[2],p[3]}; B[k]=*(AS3 bf16x8*)(bu+512*k-off); } };
      auto mmc=[&](const u32x4* A,const bf16x8* B) AI {
        #pragma unroll
        for(int k=0;k<8;k++)
        #pragma unroll
        for(int e=0;e+k<8;e++) y[e+k]=MFMA16(BC8(A[k]),B[e],y[e+k]); };
      #define C1() SG(0x8,1);SG(0x100,1);
      #define CILV() do{ C1()C1()C1()C1()C1()C1()C1()C1()C1()C1()C1()C1()C1()C1()C1()C1()C1()C1()C1()C1()C1()C1()C1()C1() SG(0x8,12);}while(0)
      ld(A0,B0,0);
      ST(3);
      SB();
      #pragma unroll 1
      for(int it=0;it<4;it++){
        ld(A1,B1,64); mmc(A0,B0); CILV(); SB();
        ak-=128; bu-=128;
        if(it==3){ ak+=64; bu+=64; }
        ld(A0,B0,0); mmc(A1,B1); CILV(); SB();
      }
    }
    ST(4);
    {
      const float dk=blo(q_dk);
      #pragma unroll
      for(int i=0;i<8;i++){
        u32x2 uu=*(AS3 u32x2*)(R+480+512*i+32*l15+8*g4);
        u32x2 o={pk2s(y[i][0]+blo(uu[0])*dk,y[i][1]+bhi(uu[0])*dk),pk2s(y[i][2]+blo(uu[1])*dk,y[i][3]+bhi(uu[1])*dk)};
        __builtin_amdgcn_raw_buffer_store_b64(o,rs,32*l15+8*g4+512*i,WS_BT+ch*4096,16);
      }
      #pragma unroll
      for(int i=0;i<10;i++) *(AS3 u32x4*)(Q+lane*16+i*1024)=WC[i];
      asm volatile("":::"memory");
      if(lane==0) __hip_atomic_fetch_add(LF(11),1u,RLX_WG);
      WAITV(0);
      if(lds_arrive(LF(10),lane)==3 && lane<16)
        __hip_atomic_store((gu32*)(ws+WS_FB+bid_g2(lane,c)*128+r*4),tag,RLX_AGENT);
    }
    ST(5);
  } else {
    const int wr=wid>>1, wh=wid&1;
    unsigned epoch=__hip_atomic_load((gu32*)(ws+WS_EPOCH), RLX_AGENT);
    if(wid==0 && lane<16) *LF(lane)=0u;
    unsigned ax[2];
    { int row=64*wr+l15; int f=(row>>1)&7;
      #pragma unroll
      for(int ks=0;ks<2;ks++) ax[ks]=row*128+(((4*ks+g4)^f)*16); }
    unsigned aw[2];
    { int q=l15>>2,p=lane&3; int sw=(((q>>1)&1)|((g4&1)<<1))<<1;
      #pragma unroll
      for(int n=0;n<2;n++){ int cc=(4*wh+2*n+(p>>1))^sw; aw[n]=16384+(8*g4+q)*128+cc*16+8*(p&1);} }
    f32x4 acc[4][6];
    #pragma unroll
    for(int m=0;m<4;m++)
    #pragma unroll
    for(int n=0;n<6;n++){ const float v=n<4?ACC0:0.f; acc[m][n]=f32x4{v,v,v,v}; }
    struct Fr{ bf16x8 x[4]; bf16x8 w[6]; };
    auto rd=[&](Fr&F,int slot,int ks) AI {
      AS3 char* b=smem+slot*SLOT;
      #pragma unroll
      for(int m=0;m<4;m++) F.x[m]=*(AS3 bf16x8*)(b+ax[ks]+m*2048);
      #pragma unroll
      for(int sg=0;sg<3;sg++)
      #pragma unroll
      for(int n=0;n<2;n++){ AS3 char* bw=b+aw[n]+ks*4096+sg*8192; F.w[sg*2+n]=CAT8(TR16(bw),TR16(bw+512)); }
    };
    auto mmn=[&](const Fr&F,int n) AI {
      #pragma unroll
      for(int m=0;m<4;m++) acc[m][n]= n<2 ? MFMA16(F.w[n],F.x[m],acc[m][n]) : MFMA16(F.x[m],F.w[n],acc[m][n]);
    };
    auto mm=[&](const Fr&F) AI {
      #pragma unroll
      for(int n=0;n<6;n++) mmn(F,n);
    };
    Fr F0,F1;
    #define I1() SG(0x8,1);SG(0x100,1);
    #define ILV() do{ I1()I1()I1()I1()I1()I1()I1()I1()I1()I1()I1()I1()I1()I1()I1()I1() SG(0x8,8);}while(0)
    const unsigned tag=__builtin_amdgcn_readfirstlane(epoch)+TAGINC;
    __builtin_amdgcn_s_waitcnt(0xC07F);
    BAR_L();
    ST(1);
    rd(F0,0,0);
    int slot=0;
    SB();
    for(int s=0;s<NST-1;s++){
      rd(F1,slot,1); mm(F0); ILV(); SB();
      slot = slot==2?0:slot+1;
      BAR_L(); SB(); rd(F0,slot,0);
      mm(F1); ILV(); SB();
    }
    rd(F1,slot,1); mm(F0); ILV(); SB();
    WAITL();
    if(lane==0) __hip_atomic_fetch_add(LF(13),1u,RLX_WG);
    ST(2);
    __builtin_amdgcn_s_setprio(2);
    SB();
    mmn(F1,2); mmn(F1,3); mmn(F1,4); mmn(F1,5); mmn(F1,0); mmn(F1,1);
    u32x4 pk[2][2], pv[2][2];
    #pragma unroll
    for(int dl=0;dl<2;dl++)
    #pragma unroll
    for(int mu=0;mu<2;mu++) pk[mu][dl]=phi8(acc[2*mu][2+dl],acc[2*mu+1][2+dl]);
    #pragma unroll
    for(int ep=0;ep<2;ep++)
    #pragma unroll
    for(int mu=0;mu<2;mu++) pv[mu][ep]=pk8(acc[2*mu][4+ep],acc[2*mu+1][4+ep]);
    SG(0x8,6);
    #define V1() SG(0x8,1);SG(0x2,3);
    V1()V1()V1()V1()V1()V1()V1()V1()V1()V1()V1()V1()V1()V1()V1()V1()V1()V1()
    SB();
    const u32x4 ones={0x3F803F80u,0x3F803F80u,0x3F803F80u,0x3F803F80u};
    AS3 char* inbox=smem+2*SLOT+wid*10240+R_IN+lane*16;
    AS3 char* outbox=smem+2*SLOT+(wid^2)*10240+R_IN+lane*16;
    {
      const u32x4 vf0=wr?pv[0][0]:pv[0][1], vf1=wr?pv[1][0]:pv[1][1], vn0=wr?pv[0][1]:pv[0][0], vn1=wr?pv[1][1]:pv[1][0];
      const f32x4 zero={};
      f32x4 s0=MFMA16(BC8(pk[0][0]),BC8(vf0),zero), s1=MFMA16(BC8(pk[0][1]),BC8(vf0),zero);
      s0=MFMA16(BC8(pk[1][0]),BC8(vf1),s0); s1=MFMA16(BC8(pk[1][1]),BC8(vf1),s1);
      f32x4 z0=MFMA16(BC8(pk[0][0]),BC8(ones),zero), z1=MFMA16(BC8(pk[0][1]),BC8(ones),zero);
      z0=MFMA16(BC8(pk[1][0]),BC8(ones),z0); z1=MFMA16(BC8(pk[1][1]),BC8(ones),z1);
      f32x4 o0=MFMA16(BC8(pk[0][0]),BC8(vn0),zero), o1=MFMA16(BC8(pk[0][1]),BC8(vn0),zero);
      o0=MFMA16(BC8(pk[1][0]),BC8(vn1),o0); o1=MFMA16(BC8(pk[1][1]),BC8(vn1),o1);
      *(AS3 u32x4*)outbox=pk8(s0,s1);
      asm volatile("":::"memory");
      __hip_atomic_store(LF(wid^2),1u,RLX_WG);
      if(l15==0){
        unsigned zo=WS_ZP+((((c*2+wh)*2+wr)*16+r)*4+g4)*32;
        STB128(__builtin_bit_cast(u32x4,z0),zo,0);
        STB128(__builtin_bit_cast(u32x4,z1),zo+16,0);
      }
      u32x4 in;
      #pragma unroll 1
      for(unsigned it=0; it<(SPINCAP<<4); ++it){
        unsigned v=__hip_atomic_load(LF(wid),RLX_WG);
        asm volatile("":::"memory");
        in=*(AS3 u32x4*)inbox;
        asm volatile("":::"memory");
        if((unsigned)__builtin_amdgcn_readfirstlane(v)>=1u) break;
      }
      u32x4 v={pk2s(o0[0]+blo(in[0]),o0[1]+bhi(in[0])),pk2s(o0[2]+blo(in[1]),o0[3]+bhi(in[1])),pk2s(o1[0]+blo(in[2]),o1[1]+bhi(in[2])),pk2s(o1[2]+blo(in[3]),o1[3]+bhi(in[3]))};
      STB128(v,lane*16,WS_KVP+(((c*2+wh)*2+wr)*16+r)*1024);
    }
    SB();
    ST(3);
    SB();
    u32x4 pq[4];
    pq[0]=phi8(acc[0][0],acc[0][1]); pq[1]=phi8(acc[1][0],acc[1][1]);
    asm volatile("s_waitcnt vmcnt(0)":"+v"(pq[0]),"+v"(pq[1])::"memory");
    const bool last1=lds_arrive(LF(6),lane)==3;
    if(last1 && lane<16) __hip_atomic_store((gu32*)(ws+WS_FKV+bid_g1(lane,cq)*128+r*4),tag,RLX_AGENT);
    ST(4);
    #pragma unroll
    for(int i=0;i<9;i++) GO(9*wid+i,L_E+(9*wid+i)*1024);
    pq[2]=phi8(acc[2][0],acc[2][1]); pq[3]=phi8(acc[3][0],acc[3][1]);
    PIN(pq[2]); PIN(pq[3]);
    u32x4 sel0={0,0,0,0}, sel1={0,0,0,0};
    { unsigned on=((l15>>2)==g4)?((l15&1)?0x3F800000u:0x00003F80u):0u;
      unsigned lo=(l15&2)?0u:on, hi=(l15&2)?on:0u;
      sel0[0]=lo; sel0[1]=hi; sel1[2]=lo; sel1[3]=hi; PIN(sel0); PIN(sel1); }
    if(last1){
      poll_all((gu32*)(ws+WS_FKV+bid*128+l15*4),tag);
      if(lane==0) __hip_atomic_store(LF(7),1u,RLX_WG);
    } else lds_wait_ge<1>(LF(7),1u);
    ST(5);
    u32x4 kvF[2], zF;
    {
      u32x4 P[16], Z[4];
      const unsigned ko=WS_KVP+((cq*2+wh)*2+wr)*16384;
      const unsigned zo=WS_ZP+(((cq*2+wh)*2)*16+l15)*128+g4*32;
      Z[0]=LDB128(zo,0); Z[1]=LDB128(zo+16,0); Z[2]=LDB128(zo+2048,0); Z[3]=LDB128(zo+2048+16,0);
      #pragma unroll
      for(int p=0;p<8;p++) P[p]=LDB128(lane*16+(p&3)*1024,ko+(((p>>2)+r)&3)*4096);
      SB();
      f32x4 d0={},d1={};
      #pragma unroll
      for(int p=0;p<8;p++){
        P[p+8]=LDB128(lane*16+(p&3)*1024,ko+(((p>>2)+2+r)&3)*4096);
        d0=MFMA16(BC8(sel0),BC8(P[p]),d0); d1=MFMA16(BC8(sel1),BC8(P[p]),d1);
        SG(0x8,2); SG(0x20,1);
      }
      SB();
      float z[8];
      #pragma unroll
      for(int i=0;i<8;i++){ float t=u2f(Z[i>>2][i&3])+u2f(Z[2+(i>>2)][i&3]); asm("":"+v"(t)); z[i]=row_sum16(t); }
      zF=u32x4{pk2s(z[0],z[1]),pk2s(z[2],z[3]),pk2s(z[4],z[5]),pk2s(z[6],z[7])};
      PIN(zF);
      SB();
      #pragma unroll
      for(int p=8;p<16;p++){ d0=MFMA16(BC8(sel0),BC8(P[p]),d0); d1=MFMA16(BC8(sel1),BC8(P[p]),d1); }
      u32x4 own=pk8(d0,d1);
      *(AS3 u32x4*)outbox=own;
      asm volatile("":::"memory");
      __hip_atomic_store(LF(wid^2),2u,RLX_WG);
      u32x4 oth;
      #pragma unroll 1
      for(unsigned it=0; it<(SPINCAP<<4); ++it){
        unsigned v=__hip_atomic_load(LF(wid),RLX_WG);
        asm volatile("":::"memory");
        oth=*(AS3 u32x4*)inbox;
        asm volatile("":::"memory");
        if((unsigned)__builtin_amdgcn_readfirstlane(v)>=2u) break;
      }
      kvF[0]=wr?oth:own; kvF[1]=wr?own:oth;
    }
    ST(6);
    {
      const f32x4 zero={};
      f32x4 den[4],n0[4],n1[4];
      #pragma unroll
      for(int m=0;m<4;m++) den[m]=MFMA16(BC8(zF),BC8(pq[m]),zero);
      #pragma unroll
      for(int m=0;m<4;m++){ n0[m]=MFMA16(BC8(kvF[0]),BC8(pq[m]),zero); n1[m]=MFMA16(BC8(kvF[1]),BC8(pq[m]),zero); }
      const unsigned vo=(g4>>1)*1024+((g4&1)*32+l15)*16;
      #pragma unroll
      for(int m=0;m<4;m++){
        float rc=__builtin_amdgcn_rcpf(den[m][0]+EPS);
        u32x4 v={pk2s(n0[m][0]*rc,n0[m][1]*rc),pk2s(n0[m][2]*rc,n0[m][3]*rc),pk2s(n1[m][0]*rc,n1[m][1]*rc),pk2s(n1[m][2]*rc,n1[m][3]*rc)};
        STB128(v,vo+(m&1)*256,WS_OF+((4*r+2*wr+(m>>1))*32+2*cq+wh)*2048);
      }
    }
    ST(7);
    lds_wait_ge<1>(LF(4),4u);
    WAITV(0);
    const bool last2 = lds_arrive(LF(8),lane)==3;
    if(last2 && lane<16) __hip_atomic_store((gu32*)(ws+WS_FO+bid_g2(r,lane)*128+c*4),tag,RLX_AGENT);
    ST(8);
    #pragma unroll
    for(int i=0;i<10;i++) GO(36+10*wid+i,SLOT+(10*wid+i)*1024);
    const int hh=lane>>5, l31=lane&31;
    unsigned ao[2];
    { int q=l15>>2,p=lane&3,blk=g4&1;
      #pragma unroll
      for(int n=0;n<2;n++) ao[n]=(4*hh+q)*128+(((4*n+2*blk+(p>>1))^(((q>>1)&1)<<2))*16)+8*(p&1); }
    AS3 char* aE[2]={smem+L_E+ao[0],smem+L_E+ao[1]};
    AS3 char* a1[2]={smem+SLOT+ao[0],smem+SLOT+ao[1]};
    AS3 char* a0[2]={smem+ao[0],smem+ao[1]};
    AS3 char* a2_[2]={smem+2*SLOT+R_WO+ao[0],smem+2*SLOT+R_WO+ao[1]};
    PIN(aE[0]);PIN(aE[1]);PIN(a1[0]);PIN(a1[1]);PIN(a0[0]);PIN(a0[1]);PIN(a2_[0]);PIN(a2_[1]);
    bf16x8 A[2][2][2];
    auto rdA=[&](int h) AI {
      #pragma unroll
      for(int ks=0;ks<2;ks++)
      #pragma unroll
      for(int n=0;n<2;n++){
        AS3 char* bw = h<9 ? aE[n]+4096*h+1024*ks : h<19 ? a1[n]+4096*(h-9)+1024*ks : h<29 ? a0[n]+4096*(h-19)+1024*ks : a2_[n]+1024*(h-29)+10240*ks;
        A[h&1][ks][n]=CAT8(TR16(bw),TR16(bw+(h>=29?20480:2048))); }
    };
    if(last2){
      poll_all((gu32*)(ws+WS_FO+bid*128+l15*4),tag);
      if(lane==0) __hip_atomic_store(LF(9),1u,RLX_WG);
      if(bid==0 && lane==0) __hip_atomic_store((gu32*)(ws+WS_EPOCH),tag,RLX_AGENT);
    } else lds_wait_ge<1>(LF(9),1u);
    ST(9);
    f32x16 a2[2]={};
    const unsigned oo=WS_OF+(4*r2+wid)*65536;
    const int t0=128*r2+32*wid;
#define PF 6
    u32x4 B[32][2];
    auto ldB=[&](int h) AI {
      const unsigned so=oo+(((h>>1)+hp)&15)*4096;
      B[h][0]=LDB128(lane*16+(h&1)*2048,so);
      B[h][1]=LDB128(lane*16+(h&1)*2048+1024,so);
    };
    #pragma unroll
    for(int h=0;h<PF;h++) ldB(h);
    rdA(0);
    __builtin_amdgcn_s_setprio(0);
    SB();
    u32x4 bt[2][2]; float gv; unsigned fb;
    #pragma unroll
    for(int h=0;h<32;h++){
      if(h==1){ if(lane==0) __hip_atomic_fetch_add(LF(5),1u,RLX_WG); SB(); }
      if(h==8){ lds_wait_ge<0>(LF(5),4u); SB(); }
      if(h==18){ lds_wait_ge<0>(LF(11),4u); SB(); }
      if(h==32-PF-6){ fb=__hip_atomic_load((gu32*)(ws+WS_FB+bid*128+l15*4),RLX_AGENT); SB(); }
      if(h==32-PF){
        if(__builtin_amdgcn_ballot_w64(fb!=tag)!=0ull) poll_all((gu32*)(ws+WS_FB+bid*128+l15*4),tag);
        asm volatile("":::"memory");
        #pragma unroll
        for(int n=0;n<2;n++)
        #pragma unroll
        for(int ks=0;ks<2;ks++) bt[n][ks]=LDB128(l31*4096+hh*16+ks*32,WS_BT+(64*c2+32*n)*4096+t0*2);
        gv=u2f(__hip_atomic_load((gu32*)(ws+WS_G+(t0+l31)*4),RLX_AGENT));
        SB();
      }
      if(h==1){ST(12);} if(h==8){ST(13);} if(h==16){ST(14);} if(h==24){ST(15);}
      if(h+1<32) rdA(h+1);
      if(h+PF<32) ldB(h+PF);
      #pragma unroll
      for(int ks=0;ks<2;ks++)
      #pragma unroll
      for(int n=0;n<2;n++) a2[n]=MFMA32(A[h&1][ks][n],BC8(B[h][ks]),a2[n]);
      SG(0x8,1);SG(0x100,3);SG(0x8,1);SG(0x100,3);SG(0x8,1);SG(0x100,2);SG(0x20,1);SG(0x8,1);SG(0x20,1);
      SB();
    }
    ST(10);
    {
      u32x4 sel[2];
      #pragma unroll
      for(int ks=0;ks<2;ks++){
        unsigned on=((l31>>3)==2*ks+hh)?((l31&1)?0x3F800000u:0x00003F80u):0u; int dw=(l31&7)>>1;
        sel[ks]=u32x4{dw==0?on:0u,dw==1?on:0u,dw==2?on:0u,dw==3?on:0u}; }
      char* op=out+(size_t)(t0+l31)*2048+c2*128+hh*16;
      const float g=gv, h1=rbf(1.f-g);
      #pragma unroll
      for(int n=0;n<2;n++){
        const f32x16 zero={};
        f32x16 bb=MFMA32(BC8(bt[n][0]),BC8(sel[0]),zero); bb=MFMA32(BC8(bt[n][1]),BC8(sel[1]),bb);
        f32x16 o=a2[n]*g+bb*h1;
        #pragma unroll
        for(int k=0;k<4;k+=2){
          unsigned ax_=pk2(o[4*k],o[4*k+1]), ay=pk2(o[4*k+2],o[4*k+3]), bx=pk2(o[4*k+4],o[4*k+5]), by=pk2(o[4*k+6],o[4*k+7]);
          auto r0=__builtin_amdgcn_permlane32_swap(ax_,bx,false,false); auto r1=__builtin_amdgcn_permlane32_swap(ay,by,false,false);
          u32x4 v={r0[0],r1[0],r0[1],r1[1]};
          *(u32x4*)(op+n*64+k*16)=v;
        }
      }
    }
  }
}
extern "C" void kernel_launch(void* const* d_in, const int* in_sizes, int n_in,
                              void* d_out, int out_size, void* d_ws, size_t ws_size,
                              hipStream_t stream) {
  (void)hipFuncSetAttribute((const void*)fused,hipFuncAttributeMaxDynamicSharedMemorySize,LDS_BYTES);
  fused<<<256,512,LDS_BYTES,stream>>>((const char*)d_in[0],(const char*)d_in[1],(const char*)d_in[2],
     (const char*)d_in[4],(const char*)d_in[5],(const char*)d_in[6],(const char*)d_in[7],
     (const char*)d_in[8],(const char*)d_in[9],(const char*)d_in[10],(char*)d_out,(char*)d_ws);
}
```
